```python
import torch

def _softmax(x, axis=-1):
    return torch.softmax(x, dim=axis)
def _gelu(x):
    return torch.nn.functional.gelu(x, approximate="tanh")
def _silu(x):
    return torch.nn.functional.silu(x)
def _sigmoid(x):
    return torch.sigmoid(x)
def _layernorm(x, axis=-1, eps=1e-5):
    m = x.mean(dim=axis, keepdim=True)
    v = x.var(dim=axis, keepdim=True, unbiased=False)
    return (x - m) / torch.sqrt(v + eps)
def _scatter_add(tgt, idx, src, axis=0):
    return tgt.clone().index_add_(axis, idx, src)
def _segment_sum(src, seg, n):
    out = torch.zeros((n,) + src.shape[1:], dtype=src.dtype)
    return out.index_add_(0, seg, src)
def _pad(x, pw):
    flat = tuple(v for pair in reversed(pw) for v in pair)
    return torch.nn.functional.pad(x, flat)

kernel_name = 'gated_mix__mha_block__swiglu_ffn__prompt'

def setup_inputs():

    _w = (lambda a: (lambda t: t.to(torch.bfloat16) if t.is_floating_point() else t)(torch.as_tensor(a)))
    def _A_setup():
        B, S, D, H = 1, 2048, 640, 32
        return {
            "x": _w((lambda *s: torch.randn(*s, dtype=torch.bfloat16) * 0.05)(B, S, D)),
            "wqkv": _w((lambda *s: torch.randn(*s, dtype=torch.bfloat16) * 0.8)(D, 3 * D)),
            "wo": _w((lambda *s: torch.randn(*s, dtype=torch.bfloat16) * 0.05)(D, D)),
        }
    _A_in = _A_setup()

    def _B_setup():
        B, S, D, F = 1, 2048, 640, 1536
        return {
            "x": _w((lambda *s: torch.randn(*s, dtype=torch.bfloat16) * 0.05)(B, S, D)),
            "w_gate": _w((lambda *s: torch.randn(*s, dtype=torch.bfloat16) * 0.1)(D, F)),
            "w_up": _w((lambda *s: torch.randn(*s, dtype=torch.bfloat16) * 0.1)(D, F)),
            "w_down": _w((lambda *s: torch.randn(*s, dtype=torch.bfloat16) * 0.1)(F, D)),
        }
    _B_in = _B_setup()

    return {
        "A_x": _A_in["x"],
        "A_wqkv": _A_in["wqkv"],
        "A_wo": _A_in["wo"],
        "B_x": _B_in["x"],
        "B_w_gate": _B_in["w_gate"],
        "B_w_up": _B_in["w_up"],
        "B_w_down": _B_in["w_down"],
        "gate_w": _w((lambda *s: torch.randn(*s, dtype=torch.bfloat16) * 0.05)(640) * 16.0),
    }

def reference(A_x, A_wqkv, A_wo, B_x, B_w_gate, B_w_up, B_w_down, gate_w):
    def _A_ref(x, wqkv, wo):
        B, S, D = x.shape
        H = 32
        Dh = D // H
        qkv = (lambda x, *d: x.permute(*d))((x @ wqkv).reshape(B, S, 3, H, Dh), 2, 0, 3, 1, 4)
        q, k, v = qkv[0], qkv[1], qkv[2]
        scores = torch.einsum("bhqd,bhkd->bhqk", q, k) * (Dh ** -0.5)
        p = _softmax(scores, axis=-1)
        o = torch.einsum("bhqk,bhkd->bhqd", p, v)
        return (lambda x, *d: x.permute(*d))(o, 0, 2, 1, 3).reshape(B, S, D) @ wo

    def _B_ref(x, w_gate, w_up, w_down):
        g = _silu(x @ w_gate)
        u = x @ w_up
        return (g * u) @ w_down

    x = A_x
    g = _sigmoid((x * gate_w).sum(-1, keepdims=True))
    a = _A_ref(x, A_wqkv, A_wo)
    b = _B_ref(x, B_w_gate, B_w_up, B_w_down)
    return g * a + (1 - g) * b
```

```cpp
#include <hip/hip_runtime.h>
#include <stdint.h>

typedef __bf16 bf16x8 __attribute__((ext_vector_type(8)));
typedef __bf16 bf16x16 __attribute__((ext_vector_type(16)));
typedef __bf16 bf16x2 __attribute__((ext_vector_type(2)));
typedef short s16x4 __attribute__((ext_vector_type(4)));
typedef short s16x8 __attribute__((ext_vector_type(8)));
typedef float f32x2 __attribute__((ext_vector_type(2)));
typedef float f32x4 __attribute__((ext_vector_type(4)));
typedef float f32x8 __attribute__((ext_vector_type(8)));
typedef float f32x16 __attribute__((ext_vector_type(16)));
typedef unsigned u32x2 __attribute__((ext_vector_type(2)));
typedef unsigned u32x4 __attribute__((ext_vector_type(4)));
#define LAS __attribute__((address_space(3)))
#define GAS __attribute__((address_space(1)))
#define DI __device__ __forceinline__

constexpr size_t WS_KV = 0;
constexpr size_t WS_Q = WS_KV + 16 * 16 * 20480;
constexpr size_t WS_H = WS_Q + 16 * 16 * 10240;
constexpr size_t WS_O = WS_H + 128 * 48 * 1024;
constexpr size_t WS_W3 = WS_O + 128 * 20 * 1024;
constexpr size_t WS_G = WS_W3 + 8 * 68 * 5120;
constexpr size_t WS_END = WS_G + 2048 * 4;

struct Params {
  const char* x; const char* wqkv; const char* wo; const char* wg; const char* wu; const char* wd; const char* gw;
  char* out; char* ws;
};

DI void glds16(const char* sbase, unsigned voff, unsigned lds_addr) {
  asm volatile("s_mov_b32 m0, %2\n\ts_nop 0\n\tglobal_load_lds_dwordx4 %0, %1" ::"v"(voff), "s"(sbase), "s"(lds_addr) : "memory");
}
template <int N> DI void wait_vm() { asm volatile("s_waitcnt vmcnt(%0)" ::"i"(N) : "memory"); }
template <int N> DI void wait_vm_bar() { asm volatile("s_waitcnt vmcnt(%0) lgkmcnt(0)\n\ts_barrier" ::"i"(N) : "memory"); }
DI void bar_lds() { asm volatile("s_waitcnt lgkmcnt(0)\n\ts_barrier" ::: "memory"); }

DI bf16x8 lds_b128(unsigned a) { return *(const LAS bf16x8*)(size_t)a; }
DI s16x4 lds_tr(unsigned a) { return __builtin_amdgcn_ds_read_tr16_b64_v4i16((LAS s16x4*)(size_t)a); }
DI bf16x8 lds_tr2(unsigned a0, unsigned a1) {
  s16x4 lo = lds_tr(a0), hi = lds_tr(a1);
  return __builtin_bit_cast(bf16x8, __builtin_shufflevector(lo, hi, 0, 1, 2, 3, 4, 5, 6, 7));
}
DI unsigned pk(float a, float b) { f32x2 v = {a, b}; return __builtin_bit_cast(unsigned, __builtin_convertvector(v, bf16x2)); }
DI int swap23(int r) { return (r & ~12) | ((r & 4) << 1) | ((r & 8) >> 1); }
#define MF32(a, b, c) __builtin_amdgcn_mfma_f32_32x32x16_bf16(a, b, c, 0, 0, 0)
#define MF16(a, b, c) __builtin_amdgcn_mfma_f32_16x16x32_bf16(a, b, c, 0, 0, 0)
#define MF8(a, b, c) __builtin_amdgcn_mfma_f32_32x32x8bf16_1k(a, b, c, 0, 0, 0)
DI f32x16 smf(bf16x8 a, f32x8 p, f32x16 c, int idx) {
  return __builtin_amdgcn_smfmac_f32_32x32x32_bf16(a, __builtin_bit_cast(bf16x16, p), c, idx, 0, 0);
}

constexpr float QSCALE = 0.22360679774997896f * 1.4426950408889634f;
constexpr float LOG2E = 1.4426950408889634f;

constexpr unsigned L1_RING = 0;
constexpr unsigned L1_REL = 131072;
constexpr unsigned L1_GW = L1_REL + 12288;
constexpr unsigned L2_RING = 0;
constexpr unsigned L2_ONES = 122880;
constexpr unsigned L2_SCR = L2_ONES + 5888;
constexpr unsigned LDS_BYTES = 160 * 1024;

constexpr int n1(int t) { return t < 10 ? 4 : (t < 20 ? 3 : 0); }

template <int MODE>
DI void qkv_stage(unsigned slot, int st, bf16x8 (&xf)[40], f32x16 (&acc)[2], const unsigned (&xa)[4], unsigned wa0, unsigned wa1, unsigned gwa) {
#pragma unroll
  for (int ks = 0; ks < 4; ++ks) {
    const bf16x8 x = lds_b128(xa[ks] + slot);
    xf[st * 4 + ks] = x;
    if constexpr (MODE == 2) {
      const bf16x8 g = lds_b128(gwa + (st * 4 + ks) * 32);
      acc[0] = MF32(g, x, acc[0]);
    } else {
      const bf16x8 w0 = lds_tr2(wa0 + slot + ks * 4096, wa0 + slot + ks * 4096 + 1024);
      const bf16x8 w1 = lds_tr2(wa1 + slot + ks * 4096, wa1 + slot + ks * 4096 + 1024);
      if constexpr (MODE == 0) { acc[0] = MF32(w0, x, acc[0]); acc[1] = MF32(w1, x, acc[1]); }
      else { acc[0] = MF32(x, w0, acc[0]); acc[1] = MF32(x, w1, acc[1]); }
    }
  }
}

struct P1Dma {
  const char* xb; const char* wqb; const char* gub;
  unsigned xo0, xo1, wo0, wo1, go0, go1, go2, lds, w;
  template <int T> DI void issue() const {
    if constexpr (T < 10) {
      const unsigned s = lds + L1_RING + (T & 3) * 32768 + w * 2048;
      glds16(xb + T * 128, xo0, s); glds16(xb + T * 128, xo1, s + 1024);
      glds16(wqb + T * 64 * 3840, wo0, s + 16384); glds16(wqb + T * 64 * 3840, wo1, s + 16384 + 1024);
    } else if constexpr (T < 20) {
      const unsigned s = lds + L1_RING + (T & 3) * 32768 + w * 3072;
      const char* b = gub + (T - 10) * 64 * 3072;
      glds16(b, go0, s); glds16(b, go1, s + 1024); glds16(b, go2, s + 2048);
    }
  }
};

template <int MODE, int S>
DI void qkv_loop(const P1Dma& dma, bf16x8 (&xf)[40], f32x16 (&acc)[2], const unsigned (&xa)[4], unsigned wa0, unsigned wa1, unsigned gwa) {
  if constexpr (S < 10) {
    wait_vm_bar<(S == 0 ? 12 : n1(S + 1) + n1(S + 2))>();
    if constexpr (S >= 1) dma.template issue<S + 3>();
    qkv_stage<MODE>((S & 3) * 32768, S, xf, acc, xa, wa0, wa1, gwa);
    qkv_loop<MODE, S + 1>(dma, xf, acc, xa, wa0, wa1, gwa);
  }
}
template <int S>
DI void gu_loop(const P1Dma& dma, const bf16x8 (&xf)[40], f32x16 (&acc)[3], const unsigned (&ga)[3]) {
  if constexpr (S < 20) {
    wait_vm_bar<n1(S + 1) + n1(S + 2)>();
    dma.template issue<S + 3>();
    constexpr unsigned slot = (S & 3) * 32768;
#pragma unroll
    for (int ks = 0; ks < 4; ++ks) {
#pragma unroll
      for (int m = 0; m < 3; ++m) {
        const bf16x8 w = lds_tr2(ga[m] + slot + ks * 3072, ga[m] + slot + ks * 3072 + 768);
        acc[m] = MF32(w, xf[(S - 10) * 4 + ks], acc[m]);
      }
    }
    gu_loop<S + 1>(dma, xf, acc, ga);
  }
}

DI void phase1(const Params& p, unsigned lds) {
  const int tid = threadIdx.x, l = tid & 63, w = __builtin_amdgcn_readfirstlane(tid >> 6);
  const int b = blockIdx.x, xcd = b & 7, idx = b >> 3;
  const int R = 8 * (xcd & 1) + (idx & 7), J = 4 * (xcd >> 1) + (idx >> 3);
  const int jt = J < 15 ? J : 14;
  const int r = l & 31, h = l >> 5, grp = (l >> 4) & 1, q = (l >> 2) & 3, pp = l & 3;
  const int sp = ((pp & 1) << 1) | (pp >> 1);
  const int tg = w & 3, ch = w >> 2;
  P1Dma dma;
  dma.lds = lds; dma.w = w;
  dma.xb = p.x + (size_t)R * 128 * 1280;
  dma.wqb = p.wqkv + jt * 256;
  dma.gub = (w < 4 ? p.wg : p.wu) + J * 192;
  {
    const int row0 = 16 * w + (l >> 3), row1 = row0 + 8;
    dma.xo0 = row0 * 1280 + (((l & 7) ^ ((row0 >> 1) & 7)) << 4);
    dma.xo1 = row1 * 1280 + (((l & 7) ^ ((row1 >> 1) & 7)) << 4);
    const int qq = (l >> 3) & 3;
    dma.wo0 = (8 * w + qq) * 3840 + (l >> 5) * 128 + (((l & 7) ^ ((qq >> 1) << 2)) << 4);
    dma.wo1 = dma.wo0 + 4 * 3840;
    const int up2 = w < 4 ? 0 : 2;
    int ci = (w & 3) * 192 + l, row = ci / 12, cp = ci - row * 12;
    dma.go0 = row * 3072 + ((cp ^ up2) << 4);
    ci += 64; row = ci / 12; cp = ci - row * 12;
    dma.go1 = row * 3072 + ((cp ^ up2) << 4);
    ci += 64; row = ci / 12; cp = ci - row * 12;
    dma.go2 = row * 3072 + ((cp ^ up2) << 4);
  }
  dma.issue<0>(); dma.issue<1>(); dma.issue<2>(); dma.issue<3>();
  const int unit = b * 3 + w;
  const bool do_rel = (w < 3) && (unit < 680);
  u32x4 rel[4];
  int rks = 0, rcu = 0;
  if (do_rel) {
    rks = unit / 10; rcu = unit - rks * 10;
    const char* src = (rks < 48 ? p.wd + (size_t)rks * 32 * 1280 : p.wo + (size_t)(rks - 48) * 32 * 1280) + rcu * 128;
#pragma unroll
    for (int i = 0; i < 4; ++i) rel[i] = *(const GAS u32x4*)(src + (8 * i + (l >> 3)) * 1280 + (l & 7) * 16);
  }
  if (J == 15 && tid < 80) *(LAS u32x4*)(size_t)(lds + L1_GW + tid * 16) = *(const GAS u32x4*)(p.gw + tid * 16);

  unsigned xa[4];
  {
    const int row = tg * 32 + swap23(r);
#pragma unroll
    for (int ks = 0; ks < 4; ++ks) xa[ks] = lds + L1_RING + row * 128 + (((2 * ks + h) ^ ((row >> 1) & 7)) << 4);
  }
  const unsigned wab = lds + L1_RING + 16384 + h * 2048 + ch * 512 + q * 128 + grp * 32 + sp * 8;
  const unsigned wa0 = wab + ((q >> 1) << 6), wa1 = wab + (((q >> 1) ^ 1) << 6);
  const unsigned gwa = lds + L1_GW + h * 16;
  bf16x8 xf[40];
  f32x16 acc[2];
#pragma unroll
  for (int i = 0; i < 16; ++i) { acc[0][i] = 0.f; acc[1][i] = 0.f; }
  char* ws = p.ws;
  if (J < 10) {
    qkv_loop<0, 0>(dma, xf, acc, xa, wa0, wa1, gwa);
    const float sc = J < 5 ? QSCALE : 1.0f;
    const int jj = J < 5 ? J : J - 5;
    char* base = ws + (J < 5 ? WS_Q : WS_KV);
    const int bstride = J < 5 ? 10240 : 20480;
#pragma unroll
    for (int n = 0; n < 2; ++n)
#pragma unroll
      for (int pc = 0; pc < 2; ++pc) {
        const int u = 16 * jj + 4 * (2 * ch + n) + 2 * pc + h;
        const int pr = u / 5, sl = u - pr * 5;
        u32x4 v;
#pragma unroll
        for (int i = 0; i < 4; ++i) v[i] = pk(acc[n][8 * pc + 2 * i] * sc, acc[n][8 * pc + 2 * i + 1] * sc);
        *(GAS u32x4*)(base + (pr * 16 + R) * bstride + tg * 2560 + sl * 512 + swap23(r) * 16) = v;
      }
  } else if (J < 15) {
    qkv_loop<1, 0>(dma, xf, acc, xa, wa0, wa1, gwa);
#pragma unroll
    for (int n = 0; n < 2; ++n) {
      const int f = 128 * (J - 10) + 32 * (2 * ch + n) + swap23(r);
      const int hd = f / 20, d = f - hd * 20;
      const int slot = (hd & 1) ? (d >= 4 ? swap23(d - 4) : 16 + d) : (d < 16 ? swap23(d) : d);
      char* dst = ws + WS_KV + ((hd >> 1) * 16 + R) * 20480 + 10240 + (hd & 1) * 5120 + tg * 1280 + h * 320 + slot * 16;
#pragma unroll
      for (int pc = 0; pc < 2; ++pc) {
        u32x4 v;
#pragma unroll
        for (int i = 0; i < 4; ++i) v[i] = pk(acc[n][8 * pc + 2 * i], acc[n][8 * pc + 2 * i + 1]);
        *(GAS u32x4*)(dst + pc * 640) = v;
      }
    }
  } else {
    qkv_loop<2, 0>(dma, xf, acc, xa, wa0, wa1, gwa);
    const float g = __builtin_amdgcn_rcpf(1.0f + __builtin_amdgcn_exp2f(-LOG2E * acc[0][0]));
    if (ch == 0 && h == 0) *(GAS float*)(ws + WS_G + (R * 128 + tg * 32 + swap23(r)) * 4) = g;
  }
  if (do_rel) {
    const unsigned rb = lds + L1_REL + w * 4096;
#pragma unroll
    for (int i = 0; i < 4; ++i) *(LAS u32x4*)(size_t)(rb + i * 1024 + l * 16) = rel[i];
    const unsigned ra = rb + (8 * (l >> 4) + q) * 128 + pp * 8;
#pragma unroll
    for (int i = 0; i < 4; ++i) {
      const bf16x8 f = lds_tr2(ra + i * 32, ra + i * 32 + 512);
      const int ft = 4 * rcu + i, cb = ft / 5, sl = ft - cb * 5;
      *(GAS bf16x8*)(ws + WS_W3 + ((cb * 68 + rks) * 5 + sl) * 1024 + l * 16) = f;
    }
  }
  unsigned ga[3];
#pragma unroll
  for (int m = 0; m < 3; ++m) ga[m] = lds + L1_RING + grp * 12288 + (8 * h + q) * 192 + (((3 * ch + m) ^ grp) << 5) + sp * 8;
  f32x16 gacc[3];
#pragma unroll
  for (int i = 0; i < 16; ++i) { gacc[0][i] = 0.f; gacc[1][i] = 0.f; gacc[2][i] = 0.f; }
  gu_loop<10>(dma, xf, gacc, ga);
  {
    const int tok = swap23(r);
    const int rt = R * 8 + tg * 2 + (tok >> 4);
#pragma unroll
    for (int m = 0; m < 3; ++m) {
      float hv[8];
#pragma unroll
      for (int j = 0; j < 8; ++j) {
        const float g = gacc[m][j], u = gacc[m][8 + j];
        hv[j] = g * __builtin_amdgcn_rcpf(1.0f + __builtin_amdgcn_exp2f(-LOG2E * g)) * u;
      }
      const u32x4 v = {pk(hv[0], hv[1]), pk(hv[2], hv[3]), pk(hv[4], hv[5]), pk(hv[6], hv[7])};
      const int P = J * 6 + ch * 3 + m;
      *(GAS u32x4*)(ws + WS_H + (rt * 48 + (P >> 1)) * 1024 + (((P & 1) * 2 + h) * 16 + (tok & 15)) * 16) = v;
    }
  }
}

__device__ __noinline__ f32x16 attn_slow(const char* kv, int hd, bf16x8 q16, s16x4 q4) {
  const int l = threadIdx.x & 63, r = l & 31, h = l >> 5;
  f32x16 o;
#pragma unroll
  for (int i = 0; i < 16; ++i) o[i] = 0.f;
  float m = -__builtin_inff();
  const u32x4 ones = {0x3f803f80u, 0x3f803f80u, 0x3f803f80u, 0x3f803f80u};
  for (int t = 0; t < 64; ++t) {
    const char* blk = kv + (t >> 2) * 20480;
    const char* kt = blk + (t & 3) * 2560;
    const bf16x8 k16 = *(const GAS bf16x8*)(kt + (hd * 3 + h) * 512 + r * 16);
    const s16x4 k4 = *(const GAS s16x4*)(kt + 1024 + r * 16 + hd * 8);
    const char* vt = blk + 10240 + hd * 5120 + (t & 3) * 1280 + h * 320 + (r < 20 ? r : 0) * 16;
    u32x4 v0 = *(const GAS u32x4*)vt, v1 = *(const GAS u32x4*)(vt + 640);
    if (r >= 20) { v0 = ones; v1 = ones; }
    f32x16 s;
#pragma unroll
    for (int i = 0; i < 16; ++i) s[i] = 0.f;
    s = MF32(k16, q16, s);
    s = MF8(k4, q4, s);
    float tm = s[0];
#pragma unroll
    for (int i = 1; i < 16; ++i) tm = fmaxf(tm, s[i]);
    tm = fmaxf(tm, __shfl_xor(tm, 32));
    const float mn = fmaxf(m, tm);
    const float alpha = __builtin_amdgcn_exp2f(m - mn);
    m = mn;
#pragma unroll
    for (int i = 0; i < 16; ++i) { o[i] *= alpha; s[i] = __builtin_amdgcn_exp2f(s[i] - mn); }
    o = smf(__builtin_bit_cast(bf16x8, v0), __builtin_shufflevector(s, s, 0, 1, 2, 3, 4, 5, 6, 7), o, 0xDDDD);
    o = smf(__builtin_bit_cast(bf16x8, v1), __builtin_shufflevector(s, s, 8, 9, 10, 11, 12, 13, 14, 15), o, 0xDDDD);
  }
  return o;
}

DI void phase2(const Params& p, unsigned lds) {
  const int tid = threadIdx.x, l = tid & 63, w = __builtin_amdgcn_readfirstlane(tid >> 6);
  const int b = blockIdx.x, xcd = b & 7, idx = b >> 3;
  const int R = idx & 15, J = 2 * xcd + (idx >> 4);
  const int r = l & 31, h = l >> 5;
  const int hd = w & 1, qh = (w >> 1) & 1, kh = w >> 2;
  const char* kv = p.ws + WS_KV + (size_t)J * 16 * 20480;
  const unsigned voff = w * 5120 + l * 16;
  auto issue = [&](int c) {
    const unsigned s = lds + L2_RING + (c & 1) * 40960 + w * 5120;
    const char* src = kv + c * 40960;
#pragma unroll
    for (int i = 0; i < 5; ++i) glds16(src + i * 1024, voff, s + i * 1024);
  };
  issue(0);
  bf16x8 q16[2]; s16x4 q4[2];
  {
    const char* qb = p.ws + WS_Q + (J * 16 + R) * 10240 + qh * 5120;
#pragma unroll
    for (int qt = 0; qt < 2; ++qt) {
      q16[qt] = *(const GAS bf16x8*)(qb + qt * 2560 + (hd * 3 + h) * 512 + r * 16);
      s16x4 t = *(const GAS s16x4*)(qb + qt * 2560 + 1024 + r * 16 + hd * 8);
      if (h) t = (s16x4){0, 0, 0, 0};
      q4[qt] = t;
    }
  }
  if (tid < 368) *(LAS u32x4*)(size_t)(lds + L2_ONES + tid * 16) = (u32x4){0x3f803f80u, 0x3f803f80u, 0x3f803f80u, 0x3f803f80u};
  f32x16 o[2];
#pragma unroll
  for (int i = 0; i < 16; ++i) { o[0][i] = 0.f; o[1][i] = 0.f; }
  const unsigned ka = lds + L2_RING + kh * 20480 + (hd * 3 + h) * 512 + r * 16;
  const unsigned k4a = lds + L2_RING + kh * 20480 + 1024 + r * 16 + hd * 8;
  const unsigned va0 = r < 20 ? lds + L2_RING + kh * 20480 + 10240 + hd * 5120 + h * 320 + r * 16
                              : lds + L2_ONES + h * 320 + (r < 28 ? 64 : 192);
  const unsigned vflip = r < 20 ? 40960u : 0u;
  int idxv = 0xDDDD;
  for (int c = 0; c < 8; ++c) {
    wait_vm_bar<0>();
    if (c < 7) issue(c + 1);
    const unsigned so = (c & 1) * 40960;
    const unsigned va = va0 + (c & 1) * vflip;
#pragma unroll
    for (int t = 0; t < 4; ++t) {
      const bf16x8 k16 = lds_b128(ka + so + t * 2560);
      const s16x4 k4 = *(const LAS s16x4*)(size_t)(k4a + so + t * 2560);
      const bf16x8 v0 = lds_b128(va + t * 1280), v1 = lds_b128(va + t * 1280 + 640);
#pragma unroll
      for (int qt = 0; qt < 2; ++qt) {
        f32x16 s;
#pragma unroll
        for (int i = 0; i < 16; ++i) s[i] = 0.f;
        s = MF32(k16, q16[qt], s);
        s = MF8(k4, q4[qt], s);
#pragma unroll
        for (int i = 0; i < 16; ++i) s[i] = __builtin_amdgcn_exp2f(s[i]);
        o[qt] = smf(v0, __builtin_shufflevector(s, s, 0, 1, 2, 3, 4, 5, 6, 7), o[qt], idxv);
        o[qt] = smf(v1, __builtin_shufflevector(s, s, 8, 9, 10, 11, 12, 13, 14, 15), o[qt], idxv);
      }
    }
  }
  f32x16 mine = kh ? o[1] : o[0], other = kh ? o[0] : o[1];
  {
    const unsigned sa = lds + L2_SCR + w * 3328 + l * 4;
#pragma unroll
    for (int i = 0; i < 13; ++i) *(LAS float*)(size_t)(sa + i * 256) = other[i];
    bar_lds();
    const unsigned ra = lds + L2_SCR + (w ^ 4) * 3328 + l * 4;
#pragma unroll
    for (int i = 0; i < 13; ++i) mine[i] += *(const LAS float*)(size_t)(ra + i * 256);
  }
  bool ok = mine[12] >= 0x1p-20f && mine[12] < 0x1p126f;
#pragma unroll
  for (int i = 0; i < 12; ++i) ok = ok && (__builtin_fabsf(mine[i]) < __builtin_inff());
  if (!__all(ok)) mine = attn_slow(kv, hd, kh ? q16[1] : q16[0], kh ? q4[1] : q4[0]);
  const float rl = __builtin_amdgcn_rcpf(mine[12]);
  const int row = R * 128 + qh * 64 + kh * 32 + r;
  char* ob = p.ws + WS_O + (row >> 4) * 20480 + (row & 15) * 16;
  {
    const u32x4 v = {pk(mine[0] * rl, mine[1] * rl), pk(mine[2] * rl, mine[3] * rl), pk(mine[4] * rl, mine[5] * rl), pk(mine[6] * rl, mine[7] * rl)};
    *(GAS u32x4*)(ob + (5 * J + 3 * hd + h) * 256) = v;
    if (h == 0) {
      const u32x2 t = {pk(mine[8] * rl, mine[9] * rl), pk(mine[10] * rl, mine[11] * rl)};
      *(GAS u32x2*)(ob + (5 * J + 2) * 256 + hd * 8) = t;
    }
  }
}

template <int S>
DI void p3_loop(const char* wsrc, unsigned voff, unsigned lds, int w, unsigned wa, const bf16x8 (&hf)[2][12], const bf16x8 (&of)[2][5], f32x4 (&ab)[2][5], f32x4 (&aa)[2][5]) {
  if constexpr (S < 9) {
    wait_vm_bar<(S == 8 ? 0 : (S == 7 ? 3 : 5))>();
    if constexpr (S + 2 < 8) {
#pragma unroll
      for (int i = 0; i < 5; ++i) glds16(wsrc + (S + 2) * 40960 + i * 1024, voff, lds + L2_RING + ((S + 2) % 3) * 40960 + w * 5120 + i * 1024);
    } else if constexpr (S + 2 == 8) {
#pragma unroll
      for (int i = 0; i < 3; ++i) {
        const int pc = (w * 3 + i < 20) ? w * 3 + i : 19;
        glds16(wsrc + 8 * 40960 + pc * 1024 - w * 5120, voff, lds + L2_RING + (8 % 3) * 40960 + pc * 1024);
      }
    }
#pragma unroll
    for (int kk = 0; kk < (S == 8 ? 1 : 2); ++kk) {
      constexpr int dummy = 0; (void)dummy;
      const int j = 2 * S + kk;
#pragma unroll
      for (int ft = 0; ft < 5; ++ft) {
        const bf16x8 wf = lds_b128(wa + (S % 3) * 40960 + kk * 20480 + ft * 1024);
        if (j < 12) { ab[0][ft] = MF16(wf, hf[0][j < 12 ? j : 0], ab[0][ft]); ab[1][ft] = MF16(wf, hf[1][j < 12 ? j : 0], ab[1][ft]); }
        else { aa[0][ft] = MF16(wf, of[0][j >= 12 ? j - 12 : 0], aa[0][ft]); aa[1][ft] = MF16(wf, of[1][j >= 12 ? j - 12 : 0], aa[1][ft]); }
      }
    }
    p3_loop<S + 1>(wsrc, voff, lds, w, wa, hf, of, ab, aa);
  }
}

DI void phase3(const Params& p, unsigned lds) {
  const int tid = threadIdx.x, l = tid & 63, w = __builtin_amdgcn_readfirstlane(tid >> 6);
  const int b = blockIdx.x, xcd = b & 7, idx = b >> 3;
  const int rb = 8 * (xcd >> 1) + (idx & 7), cb = 4 * (xcd & 1) + (idx >> 3);
  const int rg = w & 1, kq = w >> 1;
  const char* wsrc = p.ws + WS_W3 + (size_t)cb * 68 * 5120;
  const unsigned voff = w * 5120 + l * 16;
  bf16x8 hf[2][12], of[2][5];
#pragma unroll
  for (int t = 0; t < 2; ++t) {
    const int rt = rb * 4 + rg * 2 + t;
#pragma unroll
    for (int j = 0; j < 12; ++j) hf[t][j] = *(const GAS bf16x8*)(p.ws + WS_H + (rt * 48 + 4 * j + kq) * 1024 + l * 16);
#pragma unroll
    for (int j = 0; j < 5; ++j) of[t][j] = *(const GAS bf16x8*)(p.ws + WS_O + (rt * 20 + 4 * j + kq) * 1024 + l * 16);
  }
  float g[2];
#pragma unroll
  for (int t = 0; t < 2; ++t) g[t] = *(const GAS float*)(p.ws + WS_G + (rb * 64 + rg * 32 + t * 16 + (l & 15)) * 4);
#pragma unroll
  for (int s = 0; s < 2; ++s)
#pragma unroll
    for (int i = 0; i < 5; ++i) glds16(wsrc + s * 40960 + i * 1024, voff, lds + L2_RING + s * 40960 + w * 5120 + i * 1024);
  f32x4 ab[2][5], aa[2][5];
#pragma unroll
  for (int t = 0; t < 2; ++t)
#pragma unroll
    for (int ft = 0; ft < 5; ++ft) { ab[t][ft] = (f32x4){0.f, 0.f, 0.f, 0.f}; aa[t][ft] = (f32x4){0.f, 0.f, 0.f, 0.f}; }
  const unsigned wa = lds + L2_RING + kq * 5120 + l * 16;
  p3_loop<0>(wsrc, voff, lds, w, wa, hf, of, ab, aa);
  bar_lds();
#pragma unroll
  for (int t = 0; t < 2; ++t)
#pragma unroll
    for (int ft = 0; ft < 5; ++ft) {
      const f32x4 v = aa[t][ft] * g[t] + ab[t][ft] * (1.0f - g[t]);
      *(LAS f32x4*)(size_t)(lds + ((rg * 10 + t * 5 + ft) * 4 + kq) * 1024 + l * 16) = v;
    }
  bar_lds();
  for (int tile = w; tile < 20; tile += 8) {
    const unsigned a = lds + tile * 4096 + l * 16;
    const f32x4 v = (*(const LAS f32x4*)(size_t)a + *(const LAS f32x4*)(size_t)(a + 1024)) +
                    (*(const LAS f32x4*)(size_t)(a + 2048) + *(const LAS f32x4*)(size_t)(a + 3072));
    const int rtl = tile / 5, ft = tile - rtl * 5;
    const u32x2 o = {pk(v[0], v[1]), pk(v[2], v[3])};
    *(GAS u32x2*)(p.out + (size_t)(rb * 64 + rtl * 16 + (l & 15)) * 1280 + cb * 160 + ft * 32 + (l >> 4) * 8) = o;
  }
}

template <int PH>
__global__ __launch_bounds__(512, 2) void fwd(Params p) {
  extern __shared__ __attribute__((aligned(16))) char smem[];
  const unsigned lds = (unsigned)(size_t)(LAS char*)smem;
  if constexpr (PH == 1) phase1(p, lds);
  if constexpr (PH == 2) phase2(p, lds);
  if constexpr (PH == 3) phase3(p, lds);
}

extern "C" void kernel_launch(void* const* d_in, const int* in_sizes, int n_in, void* d_out, int out_size, void* d_ws, size_t ws_size,
                              hipStream_t stream) {
  Params p;
  p.x = (const char*)d_in[0]; p.wqkv = (const char*)d_in[1]; p.wo = (const char*)d_in[2];
  p.wg = (const char*)d_in[4]; p.wu = (const char*)d_in[5]; p.wd = (const char*)d_in[6]; p.gw = (const char*)d_in[7];
  p.out = (char*)d_out; p.ws = (char*)d_ws;
  (void)hipFuncSetAttribute((const void*)fwd<1>, hipFuncAttributeMaxDynamicSharedMemorySize, LDS_BYTES);
  (void)hipFuncSetAttribute((const void*)fwd<2>, hipFuncAttributeMaxDynamicSharedMemorySize, LDS_BYTES);
  (void)hipFuncSetAttribute((const void*)fwd<3>, hipFuncAttributeMaxDynamicSharedMemorySize, LDS_BYTES);
  fwd<1><<<256, 512, LDS_BYTES, stream>>>(p);
  fwd<2><<<256, 512, LDS_BYTES, stream>>>(p);
  fwd<3><<<256, 512, LDS_BYTES, stream>>>(p);
}
```

```cpp
#include <hip/hip_runtime.h>
#include <stdint.h>

typedef __bf16 bf16x8 __attribute__((ext_vector_type(8)));
typedef __bf16 bf16x16 __attribute__((ext_vector_type(16)));
typedef __bf16 bf16x2 __attribute__((ext_vector_type(2)));
typedef short s16x4 __attribute__((ext_vector_type(4)));
typedef float f32x2 __attribute__((ext_vector_type(2)));
typedef float f32x4 __attribute__((ext_vector_type(4)));
typedef float f32x8 __attribute__((ext_vector_type(8)));
typedef float f32x16 __attribute__((ext_vector_type(16)));
typedef unsigned u32x2 __attribute__((ext_vector_type(2)));
typedef unsigned u32x4 __attribute__((ext_vector_type(4)));
typedef unsigned long long u64;
typedef __amdgpu_buffer_rsrc_t rsrc_t;
#define LAS __attribute__((address_space(3)))
#define GAS __attribute__((address_space(1)))
#define DI __device__ __forceinline__
#define SB() __builtin_amdgcn_sched_barrier(0)

constexpr unsigned WS_KV = 0;
constexpr unsigned WS_Q = WS_KV + 16 * 64 * 5120;
constexpr unsigned WS_H = WS_Q + 16 * 64 * 2560;
constexpr unsigned WS_O = WS_H + 128 * 48 * 1024;
constexpr unsigned WS_W3 = WS_O + 128 * 20 * 1024;
constexpr unsigned WS_G = WS_W3 + 8 * 68 * 5120;
constexpr unsigned WS_ONES = WS_G + 2048 * 4;
constexpr unsigned WS_E = WS_ONES + 1024;
constexpr unsigned WS_F1 = WS_E + 256;
constexpr unsigned WS_F2 = WS_F1 + 2048;
constexpr unsigned WS_F3 = WS_F2 + 2048;
constexpr unsigned WS_END = WS_F3 + 2048;
constexpr unsigned OOB = 0x7ffffff0u;
constexpr int AUX_SC1 = 16;
constexpr unsigned SPIN_CAP = 1u << 18;

struct Params {
  const char* x; const char* wqkv; const char* wo; const char* wg; const char* wu; const char* wd; const char* gw;
  char* out; char* ws;
};

struct Stamps {};
#define STAMP(i)
#define STAMP_OUT(ph, n)

DI int lane_id() { int l; asm volatile("v_mbcnt_lo_u32_b32 %0, -1, 0\n\tv_mbcnt_hi_u32_b32 %0, -1, %0" : "=v"(l)); return l; }
#define LANE_VARS const int l = lane_id(), i16 = l & 15, g = l >> 4, q = (l >> 2) & 3, pp = l & 3, r = l & 31, h = l >> 5; (void)i16; (void)g; (void)q; (void)pp; (void)r; (void)h
DI void glds16(const char* sbase, unsigned voff, unsigned lds_addr) {
  asm volatile("s_mov_b32 m0, %2\n\ts_nop 0\n\tglobal_load_lds_dwordx4 %0, %1" ::"v"(voff), "s"(sbase), "s"(lds_addr) : "memory");
}
template <int N> DI void wait_vm() { asm volatile("s_waitcnt vmcnt(%0)" ::"i"(N) : "memory"); }
template <int N> DI void wait_vm_bar() { asm volatile("s_waitcnt vmcnt(%0) lgkmcnt(0)\n\ts_barrier" ::"i"(N) : "memory"); }
DI void bar_lds() { asm volatile("s_waitcnt lgkmcnt(0)\n\ts_barrier" ::: "memory"); }

template <unsigned SO, unsigned SPAN> DI unsigned rebase(unsigned a) {
  if constexpr (SO + SPAN <= 65536) return a + SO;
  else { unsigned t = a + SO; asm volatile("" : "+v"(t)); return t; }
}
DI bf16x8 lds_b128(unsigned a) { return *(const LAS bf16x8*)(size_t)a; }
DI s16x4 lds_tr(unsigned a) { return __builtin_amdgcn_ds_read_tr16_b64_v4i16((LAS s16x4*)(size_t)a); }
DI bf16x8 lds_tr2(unsigned a0, unsigned a1) {
  s16x4 lo = lds_tr(a0), hi = lds_tr(a1);
  return __builtin_bit_cast(bf16x8, __builtin_shufflevector(lo, hi, 0, 1, 2, 3, 4, 5, 6, 7));
}
DI unsigned pk(float a, float b) { f32x2 v = {a, b}; return __builtin_bit_cast(unsigned, __builtin_convertvector(v, bf16x2)); }
DI int swap23(int r) { return (r & ~12) | ((r & 4) << 1) | ((r & 8) >> 1); }
DI u32x4 pair_rows(unsigned a0, unsigned a1, unsigned b0, unsigned b1) {
  const auto s0 = __builtin_amdgcn_permlane16_swap(a0, b0, false, false);
  const auto s1 = __builtin_amdgcn_permlane16_swap(a1, b1, false, false);
  return (u32x4){s0[0], s1[0], s0[1], s1[1]};
}
DI bf16x8 ldb16(rsrc_t rs, unsigned voff, unsigned soff) { return __builtin_bit_cast(bf16x8, __builtin_amdgcn_raw_buffer_load_b128(rs, voff, soff, AUX_SC1)); }
DI s16x4 ldb8(rsrc_t rs, unsigned voff, unsigned soff) { return __builtin_bit_cast(s16x4, __builtin_amdgcn_raw_buffer_load_b64(rs, voff, soff, AUX_SC1)); }
DI float ldb4(rsrc_t rs, unsigned voff, unsigned soff) { return __builtin_bit_cast(float, __builtin_amdgcn_raw_buffer_load_b32(rs, voff, soff, AUX_SC1)); }
DI void stb16(rsrc_t rs, u32x4 v, unsigned voff, unsigned soff) { __builtin_amdgcn_raw_buffer_store_b128(v, rs, voff + soff, 0, AUX_SC1); }
DI void stb8(rsrc_t rs, u32x2 v, unsigned voff, unsigned soff) { __builtin_amdgcn_raw_buffer_store_b64(v, rs, voff, soff, AUX_SC1); }
DI void stb4(rsrc_t rs, unsigned v, unsigned voff, unsigned soff) { __builtin_amdgcn_raw_buffer_store_b32(v, rs, voff, soff, AUX_SC1); }
DI u32x4 ld16_raw(const char* sbase, unsigned voff) { u32x4 v; asm volatile("global_load_dwordx4 %0, %1, %2 sc1" : "=v"(v) : "v"(voff), "s"(sbase) : "memory"); return v; }
DI u32x2 ld8_raw(const char* sbase, unsigned voff) { u32x2 v; asm volatile("global_load_dwordx2 %0, %1, %2 sc1" : "=v"(v) : "v"(voff), "s"(sbase) : "memory"); return v; }
DI void set_flag(char* ws, unsigned off, unsigned tlo, unsigned thi) {
  __hip_atomic_store((GAS u64*)(ws + off), ((u64)thi << 32) | tlo, __ATOMIC_RELAXED, __HIP_MEMORY_SCOPE_AGENT);
}
#define MF32(a, b, c) __builtin_amdgcn_mfma_f32_32x32x16_bf16(a, b, c, 0, 0, 0)
#define MF16(a, b, c) __builtin_amdgcn_mfma_f32_16x16x32_bf16(a, b, c, 0, 0, 0)
#define MF8(a, b, c) __builtin_amdgcn_mfma_f32_32x32x8bf16_1k(a, b, c, 0, 0, 0)
DI f32x16 smf(bf16x8 a, f32x8 p, f32x16 c, int idx) {
  return __builtin_amdgcn_smfmac_f32_32x32x32_bf16(a, __builtin_bit_cast(bf16x16, p), c, idx, 0, 0);
}
#define LO8(s) __builtin_shufflevector(s, s, 0, 1, 2, 3, 4, 5, 6, 7)
#define HI8(s) __builtin_shufflevector(s, s, 8, 9, 10, 11, 12, 13, 14, 15)

constexpr float LOG2E = 1.4426950408889634f;
constexpr float QSCALE = 0.22360679774997896f * LOG2E;
constexpr unsigned LDS_BYTES = 160 * 1024;
constexpr unsigned L_REL = 131072;
constexpr unsigned L_CNT = 49152;
DI unsigned scr_of(unsigned w) { return w * 9216 + (w >= 5 ? 4096 : 0); }
constexpr unsigned slot_of(int s) {
  if (s < 10) return (s & 3) * 32768;
  const int g = (s - 10) % 6;
  return g == 0 ? 139264 : g == 1 ? 65536 : g == 2 ? 90112 : g == 3 ? 114688 : g == 4 ? 0 : 24576;
}
#define INV_POS 2
#define REL_AT 4
constexpr int EX = 3;

DI void wait16(u32x4 f, const char* base, unsigned voff, unsigned tlo, unsigned thi) {
  const u64 tag = ((u64)thi << 32) | tlo;
  u64 lo = ((u64)f[1] << 32) | f[0], hi = ((u64)f[3] << 32) | f[2], m;
  unsigned n;
  asm volatile("s_mov_b32 %3, %7\n1:\n\tv_cmp_ne_u64 vcc, %6, %0\n\tv_cmp_ne_u64 %2, %6, %1\n\ts_or_b64 vcc, vcc, %2\n\ts_cbranch_scc0 2f\n\t"
               "s_sleep 2\n\tglobal_load_dwordx2 %0, %4, %5 sc1\n\tglobal_load_dwordx2 %1, %4, %5 offset:8 sc1\n\ts_waitcnt vmcnt(0)\n\t"
               "s_sub_u32 %3, %3, 1\n\ts_cbranch_scc0 1b\n2:"
               : "+v"(lo), "+v"(hi), "=&s"(m), "=&s"(n) : "v"(voff), "s"(base), "s"(tag), "i"(SPIN_CAP) : "vcc", "scc", "memory");
}

struct P1 {
  const char* xb; const char* wqb; const char* gub; char* ws;
  rsrc_t rs;
  unsigned wstride, xo0, xo1, wo0, wo1, go0, go1, go2, lds, w;
  unsigned xa[2], wa[4], ga[3];
  unsigned reldst[2]; bool relok[2];
  unsigned tlo, thi, f1off;
  u32x2 ep; u32x4 pf0, pf1;
  bf16x8 xf[40], wf[3][2];
  f32x4 acc[4][2], gacc[6][2];

  template <int T> DI void dma() const {
    if constexpr (T < 10) {
      const unsigned s = lds + slot_of(T) + w * 2048;
      glds16(xb + T * 128, xo0, s); glds16(xb + T * 128, xo1, s + 1024);
      const char* wb = wqb + T * wstride;
      glds16(wb, wo0, s + 16384); glds16(wb, wo1, s + 16384 + 1024);
    } else {
      const unsigned s = lds + slot_of(T) + w * 3072;
      const char* gb = gub + (T - 10) * 64 * 3072;
      glds16(gb, go0, s); glds16(gb, go1, s + 1024); glds16(gb, go2, s + 2048);
    }
  }
  static constexpr int stage_of(int gi) { return gi < 40 ? gi >> 2 : 10 + (gi - 40) / 6; }
  static constexpr bool first_of(int gi) { return gi < 40 ? (gi & 3) == 0 : (gi - 40) % 6 == 0; }
  template <int S> DI void sync() {
    if constexpr (S == 0) wait_vm_bar<12 + EX>();
    else if constexpr (S <= 3) wait_vm_bar<8 + EX>();
    else if constexpr (S == 4) {
      asm volatile("s_waitcnt vmcnt(8) lgkmcnt(0)\n\ts_barrier" : "+v"(ep)::"memory");
      const u64 t = (((u64)(unsigned)__builtin_amdgcn_readfirstlane(ep[1]) << 32) | (unsigned)__builtin_amdgcn_readfirstlane(ep[0])) + 0x9E3779B97F4A7C15ull;
      tlo = (unsigned)t; thi = (unsigned)(t >> 32);
    } else if constexpr (S <= 7) wait_vm_bar<8>();
    else if constexpr (S == 8) wait_vm_bar<10>();
    else if constexpr (S <= 11) wait_vm_bar<12>();
    else if constexpr (S == 12) wait_vm_bar<9>();
    else if constexpr (S == 13) {
      wait_vm_bar<0>();
      if (w == 0 && lane_id() == 0) set_flag(ws, f1off, tlo, thi);
    } else if constexpr (S == 14) bar_lds();
    else if constexpr (S == 16) wait_vm_bar<9>();
    else if constexpr (S == 17) {
      asm volatile("s_waitcnt vmcnt(0)" : "+v"(pf0), "+v"(pf1)::"memory");
      if (w == 7) {
        const int l = lane_id();
        wait16(pf0, ws + WS_F1, l * 16, tlo, thi); wait16(pf1, ws + WS_F1 + 1024, l * 16, tlo, thi);
      }
      if (w == 0) *(LAS unsigned*)(size_t)(lds + L_CNT) = 0u;
      bar_lds();
    }
  }
  template <int S> DI void after_sync() {
    if constexpr (S >= 1 && S <= 6) dma<S + 3>();
    if constexpr (S == REL_AT) {
      LANE_VARS;
      const unsigned relrd = lds + L_REL + (w >> 2) * 4096 + (8 * g + q) * 128 + (w & 3) * 32 + pp * 8;
#pragma unroll
      for (int i = 0; i < 2; ++i)
        if (relok[i]) {
          const unsigned a = relrd + i * 8192;
          stb16(rs, __builtin_bit_cast(u32x4, lds_tr2(a, a + 512)), l * 16, reldst[i]);
        }
    }
    if constexpr (S == 7) { dma<10>(); dma<11>(); }
    if constexpr (S == 8) { dma<12>(); dma<13>(); }
    if constexpr (S == 9) dma<14>();
    if constexpr (S == 10) dma<15>();
    if constexpr (S == 13) { dma<16>(); dma<17>(); dma<18>(); }
    if constexpr (S == 14) dma<19>();
    if constexpr (S == 16) {
      pf0 = (u32x4){0u, 0u, 0u, 0u}; pf1 = pf0;
      if (w == 7) { const int l = lane_id(); pf0 = ld16_raw(ws + WS_F1, l * 16); pf1 = ld16_raw(ws + WS_F1 + 1024, l * 16); }
    }
  }
  template <int MODE, int GI> DI void reads() {
    constexpr unsigned so = slot_of(stage_of(GI));
    if constexpr (GI < 40) {
      constexpr int ks = (GI >> 1) & 1, hf = GI & 1;
      if constexpr (hf == 0) { const unsigned a = rebase<so, 4096>(xa[ks]); xf[(GI >> 1) * 2] = lds_b128(a); xf[(GI >> 1) * 2 + 1] = lds_b128(a + 2048); }
      if constexpr (MODE == 2) { if constexpr (hf == 0) wf[GI % 3][0] = lds_b128(wa[0] + so + ks * 64); }
      else {
#pragma unroll
        for (int n = 0; n < 2; ++n) { const unsigned a = rebase<so + ks * 8192, 2048>(wa[2 * hf + n]); wf[GI % 3][n] = lds_tr2(a, a + 1024); }
      }
    } else {
      constexpr int ks = ((GI - 40) % 6) / 3, t = (GI - 40) % 3;
      const unsigned a = rebase<so + ks * 6144, 16384>(ga[t]);
#pragma unroll
      for (int n = 0; n < 2; ++n) wf[GI % 3][n] = lds_tr2(a + n * 12288, a + n * 12288 + 768);
    }
  }
  template <int MODE, int GI> DI void mfma() {
    const f32x4 z = {0.f, 0.f, 0.f, 0.f};
    if constexpr (GI < 40) {
      constexpr int hf = GI & 1, kk = GI >> 1;
      if constexpr (MODE == 2) {
        if constexpr (hf == 0) {
#pragma unroll
          for (int m = 0; m < 2; ++m) acc[0][m] = MF16(wf[GI % 3][0], xf[kk * 2 + m], kk == 0 ? z : acc[0][m]);
        }
      } else {
#pragma unroll
        for (int n = 0; n < 2; ++n)
#pragma unroll
          for (int m = 0; m < 2; ++m) {
            const f32x4 c = kk == 0 ? z : acc[2 * hf + n][m];
            acc[2 * hf + n][m] = MODE == 0 ? MF16(wf[GI % 3][n], xf[kk * 2 + m], c) : MF16(xf[kk * 2 + m], wf[GI % 3][n], c);
          }
      }
    } else {
      constexpr int kk = (GI - 40) / 3, t = (GI - 40) % 3;
#pragma unroll
      for (int n = 0; n < 2; ++n)
#pragma unroll
        for (int m = 0; m < 2; ++m) gacc[3 * n + t][m] = MF16(wf[GI % 3][n], xf[kk * 2 + m], kk == 0 ? z : gacc[3 * n + t][m]);
    }
  }
  template <int MODE, int GI, int END> DI void run() {
    if constexpr (GI < END) {
      constexpr int NX = GI + 2;
      if constexpr (NX < 100) {
        if constexpr (first_of(NX)) sync<stage_of(NX)>();
        reads<MODE, NX>();
      }
      SB();
      mfma<MODE, GI>();
      SB();
      if constexpr (NX < 100 && first_of(NX)) { after_sync<stage_of(NX)>(); SB(); }
      run<MODE, GI + 1, END>();
    }
  }
  template <int MODE> DI void qkv(Stamps& st) {
    sync<0>();
    STAMP(2);
    reads<MODE, 0>(); reads<MODE, 1>();
    run<MODE, 0, 14>();
    STAMP(3);
    run<MODE, 14, 26>();
    STAMP(4);
    run<MODE, 26, 40>();
  }
};

constexpr int NH = 14;
constexpr int T_PUB = 8, T_W = 9, T_H = 34;
struct Attn {
  f32x16 s[3], o[4];
  bf16x8 q16[4], k16[2], v0[2], v1[2];
  s16x4 q4[4], k4[2];
  bf16x8 hf[2][12];
  rsrc_t rs;
  char* ws;
  unsigned ko, k4o, vo, vsel;
  unsigned tb, rot;
  unsigned lds, l16, w;
  unsigned tlo, thi, f2off, f2poll, hbase;
  unsigned w3off, wdst, scr, zoff;
  float zq;
  u32x4 pf, wst[10];
  int idxv;
  DI unsigned toff(int i) const { return tb + ((rot + i) & 15) * 5120; }
  template <int I> DI void loadk() { const unsigned so = toff(I); k16[I & 1] = ldb16(rs, ko, so); k4[I & 1] = ldb8(rs, k4o, so); }
  template <int I> DI void loadv() { const unsigned va = __umul24(vsel, toff(I)) + vo; v0[I & 1] = ldb16(rs, va, 0); v1[I & 1] = ldb16(rs, va + 640, 0); }
  template <int B, int E> DI void exp4() {
#pragma unroll
    for (int i = E; i < E + 4; ++i) s[B][i] = __builtin_amdgcn_exp2f(s[B][i]);
  }
  template <int N> DI void loadh() { hf[N & 1][N >> 1] = ldb16(rs, l16, hbase + (N & 1) * 49152 + (N >> 1) * 4096); }
  template <int T> DI void side() {
    if constexpr (T == T_PUB) {
      unsigned t0; u64 sv;
      asm volatile("s_waitcnt vmcnt(0)\n\ts_mov_b64 %1, exec\n\ts_mov_b64 exec, 1\n\tv_mov_b32 %0, 1\n\tds_add_rtn_u32 %0, %2, %0\n\ts_waitcnt lgkmcnt(0)\n\t"
                   "v_cmp_eq_u32 vcc, 7, %0\n\ts_and_b64 exec, exec, vcc\n\ts_cbranch_execz 1f\n\tglobal_store_dwordx2 %3, %4, %5 sc1\n1:\n\ts_mov_b64 exec, %1"
                   : "=&v"(t0), "=&s"(sv) : "v"(lds + L_CNT), "v"(f2off), "v"(((u64)thi << 32) | tlo), "s"(ws) : "vcc", "scc", "memory");
    }
    if constexpr (T >= T_W && T < T_W + 10) {
      constexpr int i = T - T_W;
      wst[i] = __builtin_amdgcn_raw_buffer_load_b128(rs, l16, w3off + (i / 5) * 40960 + (i % 5) * 1024, AUX_SC1);
    }
    if constexpr (T == T_W + 8) {
      unsigned t0, n, c;
      asm volatile("s_mov_b32 %1, %4\n1:\n\tds_read_b32 %0, %3\n\ts_waitcnt lgkmcnt(0)\n\tv_readfirstlane_b32 %2, %0\n\ts_cmp_ge_u32 %2, 8\n\ts_cbranch_scc1 2f\n\t"
                   "s_sleep 1\n\ts_sub_u32 %1, %1, 1\n\ts_cbranch_scc0 1b\n2:"
                   : "=&v"(t0), "=&s"(n), "=&s"(c) : "v"(lds + L_CNT), "i"(SPIN_CAP) : "scc", "memory");
    }
    if constexpr (T == T_W + 8) { wdst = lds + 81920 + w * 5120 + l16; asm volatile("" : "+v"(wdst)); }
    if constexpr (T == 63) { scr = lds + scr_of(w) + l16; asm volatile("" : "+v"(scr)); }
    if constexpr (T >= T_W + 8 && T < T_W + 18) {
      constexpr int i = T - T_W - 8;
      *(LAS u32x4*)(size_t)(wdst + (i / 5) * 40960 + (i % 5) * 1024) = wst[i];
    }
    if constexpr (T == T_H - 5) pf = __builtin_amdgcn_raw_buffer_load_b128(rs, (lane_id() & 7) * 16, f2poll, AUX_SC1);
    if constexpr (T == T_H - 1) wait16(pf, ws + f2poll, (lane_id() & 7) * 16, tlo, thi);
    if constexpr (T >= T_H && T < T_H + NH) loadh<T - T_H>();
    if constexpr (T == 57) zq = ldb4(rs, (lane_id() & 31) * 4, zoff);
    if constexpr (T == 59) asm volatile("s_cmp_lg_u32 %0, 7\n\ts_cbranch_scc1 1f\n\tbuffer_inv sc1\n1:" ::"s"(w) : "scc", "memory");
  }
  template <int T> DI void slot() {
    constexpr int i = T >> 2, j = T & 3, c = T % 3, e = (T + 2) % 3, m = (T + 1) % 3, im = (T - 2) >> 2, jm = (T - 2) & 3;
    constexpr bool QK = T < 64, EXP = T >= 1 && T <= 64, PV = T >= 2;
    if constexpr (T == 52) { __builtin_amdgcn_s_setprio(0); SB(); }
    if constexpr (QK) { const f32x16 z = {0.f, 0.f, 0.f, 0.f, 0.f, 0.f, 0.f, 0.f, 0.f, 0.f, 0.f, 0.f, 0.f, 0.f, 0.f, 0.f}; s[c] = MF32(k16[i & 1], q16[j], z); }
    SB();
    side<T>();
    if constexpr (j == 0 && i + 1 < 16) loadk<i + 1>();
    if constexpr (j == 2 && i + 1 < 16) loadv<i + 1>();
    if constexpr (EXP) exp4<e, 0>();
    SB();
    if constexpr (QK) s[c] = MF8(k4[i & 1], q4[j], s[c]);
    SB();
    if constexpr (EXP) exp4<e, 4>();
    SB();
    if constexpr (PV) o[jm] = smf(v0[im & 1], LO8(s[m]), o[jm], idxv);
    SB();
    if constexpr (EXP) exp4<e, 8>();
    SB();
    if constexpr (PV) o[jm] = smf(v1[im & 1], HI8(s[m]), o[jm], idxv);
    SB();
    if constexpr (EXP) exp4<e, 12>();
    if constexpr (T >= 63) {
      constexpr int jj = T - 63;
#pragma unroll
      for (int u = 0; u < 3; ++u) *(LAS f32x4*)(size_t)(scr + jj * 3072 + u * 1024) = (f32x4){o[jj][4 * u], o[jj][4 * u + 1], o[jj][4 * u + 2], o[jj][4 * u + 3]};
    }
    SB();
  }
  template <int T> DI void run() { if constexpr (T < 66) { slot<T>(); run<T + 1>(); } }
  template <int N> DI void loadh_rest() { if constexpr (N < 24) { loadh<N>(); loadh_rest<N + 1>(); } }
};

constexpr unsigned slot3(int s) { return ((s + 2) & 3) * 40960; }
struct P3 {
  const char* wsrc; char* ws;
  unsigned voff, lds, w, l, wa, tlo, thi, f3off, f3poll, obase;
  bf16x8 wf[2][5];
  u32x4 of[2][5], pf;
  f32x4 acc[2][5];
  template <int S> DI void dma() const {
    if constexpr (S < 8) {
#pragma unroll
      for (int i = 0; i < 5; ++i) glds16(wsrc + S * 40960 + i * 1024, voff, lds + slot3(S) + w * 5120 + i * 1024);
    } else {
#pragma unroll
      for (int i = 0; i < 3; ++i) {
        const unsigned pc = (w * 3 + i < 20) ? w * 3 + i : 19;
        glds16(wsrc + 8 * 40960 + pc * 1024 - w * 5120, voff, lds + slot3(8) + pc * 1024);
      }
    }
  }
  template <int S> DI void sync() {
    if constexpr (S == 2) { wait_vm_bar<0>(); if (w == 0 && l == 0) set_flag(ws, f3off, tlo, thi); }
    if constexpr (S == 3 || S == 5) bar_lds();
    if constexpr (S == 4) {
      asm volatile("s_waitcnt vmcnt(5) lgkmcnt(0)\n\ts_barrier" : "+v"(pf)::"memory");
      wait16(pf, ws + f3poll, (l & 7) * 16, tlo, thi);
    }
    if constexpr (S == 6) {
      asm volatile("s_waitcnt vmcnt(8) lgkmcnt(0)\n\ts_barrier"
                   : "+v"(of[0][0]), "+v"(of[0][1]), "+v"(of[0][2]), "+v"(of[0][3]), "+v"(of[0][4]), "+v"(of[1][0]), "+v"(of[1][1]), "+v"(of[1][2]), "+v"(of[1][3]), "+v"(of[1][4])::"memory");
    }
    if constexpr (S == 7) wait_vm_bar<3>();
    if constexpr (S == 8) wait_vm_bar<0>();
  }
  template <int S> DI void after_sync() {
    if constexpr (S == 2) { dma<4>(); dma<5>(); }
    if constexpr (S == 3) { pf = ld16_raw(ws + f3poll, (l & 7) * 16); dma<6>(); }
    if constexpr (S == 4) {
#pragma unroll
      for (int j = 0; j < 5; ++j)
#pragma unroll
        for (int t = 0; t < 2; ++t) of[t][j] = ld16_raw(ws + obase + t * 20480 + j * 4096, l * 16);
      dma<7>();
    }
    if constexpr (S == 5) dma<8>();
  }
  template <int J> DI void reads() {
    const unsigned a = rebase<slot3(J >> 1) + (J & 1) * 20480, 5120>(wa);
#pragma unroll
    for (int ft = 0; ft < 5; ++ft) wf[J & 1][ft] = lds_b128(a + ft * 1024);
  }
  template <int J> DI void run(const bf16x8 (&hf)[2][12]) {
    if constexpr (J < 17) {
      constexpr int NX = J + 1;
      if constexpr (NX < 17) {
        if constexpr ((NX & 1) == 0) sync<NX / 2>();
        reads<NX>();
      }
      SB();
      const f32x4 z = {0.f, 0.f, 0.f, 0.f};
#pragma unroll
      for (int ft = 0; ft < 5; ++ft)
#pragma unroll
        for (int t = 0; t < 2; ++t) {
          bf16x8 a;
          if constexpr (J < 12) a = hf[t][J]; else a = __builtin_bit_cast(bf16x8, of[t][J - 12]);
          acc[t][ft] = MF16(wf[J & 1][ft], a, J == 0 ? z : acc[t][ft]);
        }
      SB();
      if constexpr (NX < 17 && (NX & 1) == 0) { after_sync<NX / 2>(); SB(); }
      run<J + 1>(hf);
    }
  }
};

__global__ __launch_bounds__(512, 2) void fwd(Params p) {
  extern __shared__ __attribute__((aligned(16))) char smem[];
  Stamps st; STAMP(0);
  const unsigned lds = (unsigned)(size_t)(LAS char*)smem;
  const int w = __builtin_amdgcn_readfirstlane(threadIdx.x >> 6);
  const int b = blockIdx.x, xcd = b & 7, idx = b >> 3;
  char* ws = p.ws;
  const rsrc_t rs = __builtin_amdgcn_make_buffer_rsrc((void*)ws, 0, (int)WS_END, 0x00020000);
  Attn a;
  unsigned tlo, thi;
  float zq;
  const int R2 = idx & 15, J2 = 2 * xcd + (idx >> 4), hd = w & 1, kq = w >> 1;
  const int rb = 8 * (xcd >> 1) + (idx & 7), cb = 4 * (xcd & 1) + (idx >> 3);
  {
    const int R = 8 * (xcd & 1) + (idx & 7), J = 4 * (xcd >> 1) + (idx >> 3);
    const int tg = w & 3, ch = w >> 2;
    P1 s;
    s.lds = lds; s.w = w; s.ws = ws; s.rs = rs;
    {
    LANE_VARS;
    s.xb = p.x + (size_t)R * 128 * 1280;
    s.wqb = J < 15 ? p.wqkv + J * 256 : p.gw;
    s.wstride = J < 15 ? 64 * 3840 : 128;
    s.gub = (w < 4 ? p.wg : p.wu) + J * 192;
    {
      const int row0 = 16 * w + (l >> 3), row1 = row0 + 8;
      s.xo0 = row0 * 1280 + (((l & 7) ^ ((row0 >> 1) & 7)) << 4);
      s.xo1 = row1 * 1280 + (((l & 7) ^ ((row1 >> 1) & 7)) << 4);
      const int f = g | ((w & 1) << 2);
      s.wo0 = J < 15 ? (8 * w + g) * 3840 + ((i16 ^ (2 * f)) << 4) : (l & 7) * 16;
      s.wo1 = J < 15 ? s.wo0 + 4 * 3840 : s.wo0;
    }
    s.dma<0>(); s.dma<1>(); s.dma<2>(); s.dma<3>();
#pragma unroll
    for (int i = 0; i < 2; ++i) {
      const int pi = (i == 1 && w < 4) ? w + 8 : w, uu = pi >> 2;
      const int u0 = 3 * b + uu, u = u0 < 680 ? u0 : 679;
      const int rks = u / 10, rcu = u - rks * 10;
      const char* src = (rks < 48 ? p.wd + (size_t)rks * 32 * 1280 : p.wo + (size_t)(rks - 48) * 32 * 1280) + rcu * 128 + (pi & 3) * 8 * 1280;
      glds16(src, (l >> 3) * 1280 + (l & 7) * 16, lds + L_REL + pi * 1024);
      const int ft = 4 * rcu + (pi & 3), c5 = ft / 5, sl = ft - c5 * 5;
      s.reldst[i] = WS_W3 + ((c5 * 68 + rks) * 5 + sl) * 1024;
      s.relok[i] = u0 < 680 && (i == 0 || w < 4);
    }
    s.ep = ld8_raw(ws + WS_E, 0);
    s.f1off = WS_F1 + b * 8;
    {
      int ci = (w & 3) * 192 + l, row = ci / 12, cp = ci - row * 12;
      s.go0 = row * 3072 + ((cp ^ ((row >> 2) & 2)) << 4);
      ci += 64; row = ci / 12; cp = ci - row * 12;
      s.go1 = row * 3072 + ((cp ^ ((row >> 2) & 2)) << 4);
      ci += 64; row = ci / 12; cp = ci - row * 12;
      s.go2 = row * 3072 + ((cp ^ ((row >> 2) & 2)) << 4);
    }
#pragma unroll
    for (int ks = 0; ks < 2; ++ks) s.xa[ks] = lds + (tg * 32 + i16) * 128 + (((4 * ks + g) ^ (i16 >> 1)) << 4);
#pragma unroll
    for (int n = 0; n < 4; ++n) s.wa[n] = lds + 16384 + (8 * g + q) * 256 + ((((ch ^ (g & 1)) << 2) | (n ^ q)) << 5) + pp * 8;
#pragma unroll
    for (int t = 0; t < 3; ++t) s.ga[t] = lds + (8 * g + q) * 192 + (((3 * ch + t) ^ (g & 1)) << 5) + pp * 8;
    if (J == 15) s.wa[0] = lds + 16384 + g * 16;
    }
    STAMP(1);
    if (J < 10) {
      s.qkv<0>(st);
      STAMP(5);
      LANE_VARS;
      const float sc = J < 5 ? QSCALE : 1.0f;
      const int jj = J < 5 ? J : J - 5;
      const unsigned base = J < 5 ? WS_Q : WS_KV, tstride = J < 5 ? 2560 : 5120;
#pragma unroll
      for (int n = 0; n < 4; ++n) {
        const int u = 16 * jj + 2 * (4 * ch + n) + h;
        const int pr = u / 5, sl = u - pr * 5;
        const f32x4 c0 = s.acc[n][0] * sc, c1 = s.acc[n][1] * sc;
        stb16(rs, pair_rows(pk(c0[0], c0[1]), pk(c0[2], c0[3]), pk(c1[0], c1[1]), pk(c1[2], c1[3])), (pr * 64 + R * 4 + tg) * tstride + sl * 512 + r * 16, base);
      }
    } else if (J < 15) {
      s.qkv<1>(st);
      STAMP(5);
      LANE_VARS;
#pragma unroll
      for (int n = 0; n < 4; ++n) {
        const int f = 128 * (J - 10) + 16 * (4 * ch + n) + i16;
        const int hh = f / 20, d = f - hh * 20;
        const int slot = (hh & 1) ? (d >= 4 ? swap23(d - 4) : 16 + d) : (d < 16 ? swap23(d) : d);
        const f32x4 c0 = s.acc[n][0], c1 = s.acc[n][1];
        stb16(rs, (u32x4){pk(c0[0], c0[1]), pk(c0[2], c0[3]), pk(c1[0], c1[1]), pk(c1[2], c1[3])},
              ((hh >> 1) * 64 + R * 4 + tg) * 5120 + 2560 + (hh & 1) * 1280 + g * 320 + slot * 16, WS_KV);
      }
    } else {
      s.qkv<2>(st);
      STAMP(5);
      LANE_VARS;
      if (ch == 0 && l < 32) stb4(rs, __builtin_bit_cast(unsigned, g ? s.acc[0][1][0] : s.acc[0][0][0]), (R * 128 + tg * 32 + l) * 4, WS_G);
      if (R == 0 && w == 4) stb16(rs, (u32x4){0x3f803f80u, 0x3f803f80u, 0x3f803f80u, 0x3f803f80u}, l * 16, WS_ONES);
    }
    STAMP(6);
    s.run<0, 40, 56>();
    STAMP(7);
    s.run<0, 56, 80>();
    STAMP(8);
    s.run<0, 80, 81>();
    STAMP(9);
    tlo = s.tlo; thi = s.thi;
    float z0, z1;
    {
    LANE_VARS;
    a.rs = rs; a.ws = ws; a.lds = lds; a.l16 = l * 16; a.w = w; a.tlo = tlo; a.thi = thi;
    const int tokp = ((r >> 2) & 1) * 16 + (r >> 3) * 4 + (r & 3);
    a.ko = (hd * 3 + h) * 512 + tokp * 16;
    a.k4o = h ? OOB : 1024 + tokp * 16 + hd * 8;
    a.vsel = r < 20 ? 1u : 0u;
    asm volatile("" : "+v"(a.vsel));
    a.vo = r < 20 ? 2560 + hd * 1280 + h * 320 + r * 16 : (r == 20 ? WS_ONES : OOB);
    a.tb = WS_KV + (J2 * 64 + kq * 16) * 5120;
    a.rot = R2;
    a.idxv = 0xDDDD;
    a.zoff = WS_G + (R2 * 128 + kq * 32) * 4;
    a.f2off = WS_F2 + (R * 16 + J) * 8;
    a.f2poll = WS_F2 + (rb >> 1) * 128;
    a.hbase = WS_H + ((rb * 4 + hd * 2) * 48 + kq) * 1024;
    a.w3off = WS_W3 + cb * 68 * 5120 + w * 5120;

    z0 = ldb4(rs, i16 * 4, WS_G + (R * 128 + tg * 32) * 4); z1 = ldb4(rs, i16 * 4 + 64, WS_G + (R * 128 + tg * 32) * 4);
    a.loadk<0>();
#pragma unroll
    for (int j = 0; j < 4; ++j) {
      const unsigned so = WS_Q + (J2 * 64 + R2 * 4 + ((kq + 1 + j) & 3)) * 2560;
      a.q16[j] = ldb16(rs, (hd * 3 + h) * 512 + r * 16, so);
      a.q4[j] = ldb8(rs, h ? OOB : 1024 + r * 16 + hd * 8, so);
    }
    a.loadv<0>();
    }
    SB();
    s.run<0, 81, 100>();
    STAMP(10);
    LANE_VARS;
    const float c[2] = {fminf(1.0f + __builtin_amdgcn_exp2f(LOG2E * z0), 3.0e38f), fminf(1.0f + __builtin_amdgcn_exp2f(LOG2E * z1), 3.0e38f)};
#pragma unroll
    for (int t = 0; t < 3; ++t) {
      float hv[2][4];
#pragma unroll
      for (int m = 0; m < 2; ++m)
#pragma unroll
        for (int e = 0; e < 4; ++e) {
          const float gt = s.gacc[t][m][e], up = s.gacc[3 + t][m][e];
          hv[m][e] = gt * up * __builtin_amdgcn_rcpf(__builtin_fmaf(__builtin_amdgcn_exp2f(-LOG2E * gt), c[m], c[m]));
        }
      const int P = J * 6 + ch * 3 + t;
      stb16(rs, pair_rows(pk(hv[0][0], hv[0][1]), pk(hv[0][2], hv[0][3]), pk(hv[1][0], hv[1][1]), pk(hv[1][2], hv[1][3])),
            (g & 1) * 49152 + (h * 16 + i16) * 16, WS_H + ((R * 8 + tg * 2) * 48 + (P >> 1)) * 1024 + (P & 1) * 512);
    }
  }
  STAMP(11);
#pragma unroll
  for (int i = 0; i < 16; ++i) { a.o[0][i] = 0.f; a.o[1][i] = 0.f; a.o[2][i] = 0.f; a.o[3][i] = 0.f; }
  if (w >= 4) __builtin_amdgcn_s_setprio(1);
  a.run<0>();
  STAMP(12);
  wait_vm<0>();
  LANE_VARS;
  zq = a.zq;
  a.loadh_rest<NH>();
  bar_lds();
  STAMP(13);
  f32x16 mine = a.o[3];
#pragma unroll
  for (int d = 1; d < 4; ++d) {
    const unsigned ra = lds + scr_of(((kq + d) & 3) * 2 + hd) + (3 - d) * 3072 + l * 16;
#pragma unroll
    for (int u = 0; u < 3; ++u) {
      const f32x4 t = *(const LAS f32x4*)(size_t)(ra + u * 1024);
#pragma unroll
      for (int k = 0; k < 4; ++k) mine[4 * u + k] += t[k];
    }
  }
  float lsum = __shfl(mine[8], r + 32);
  bool ok = lsum >= 0x1p-20f && lsum < 0x1p126f;
#pragma unroll
  for (int i = 0; i < 12; ++i) ok = ok && (__builtin_fabsf(mine[i]) < __builtin_inff());
  if (!__all(ok)) {
    f32x16 o;
#pragma unroll
    for (int i = 0; i < 16; ++i) o[i] = 0.f;
    float mx = -__builtin_inff();
#pragma unroll 1
    for (int t = 0; t < 64; ++t) {
      const unsigned so = WS_KV + (J2 * 64 + t) * 5120;
      const bf16x8 k16 = ldb16(rs, a.ko, so);
      const s16x4 k4 = ldb8(rs, a.k4o, so);
      const unsigned va = a.vo + __umul24(a.vsel, so);
      const bf16x8 v0 = ldb16(rs, va, 0), v1 = ldb16(rs, va + 640, 0);
      f32x16 sv;
#pragma unroll
      for (int i = 0; i < 16; ++i) sv[i] = 0.f;
      sv = MF32(k16, a.q16[3], sv);
      sv = MF8(k4, a.q4[3], sv);
      float tm = sv[0];
#pragma unroll
      for (int i = 1; i < 16; ++i) tm = fmaxf(tm, sv[i]);
      tm = fmaxf(tm, __shfl_xor(tm, 32));
      const float mn = fmaxf(mx, tm);
      const float alpha = __builtin_amdgcn_exp2f(mx - mn);
      mx = mn;
#pragma unroll
      for (int i = 0; i < 16; ++i) { o[i] *= alpha; sv[i] = __builtin_amdgcn_exp2f(sv[i] - mn); }
      o = smf(v0, LO8(sv), o, a.idxv);
      o = smf(v1, HI8(sv), o, a.idxv);
    }
    mine = o;
    lsum = __shfl(o[8], r + 32);
  }
  {
    const float rl = __builtin_amdgcn_rcpf(lsum) * __builtin_amdgcn_rcpf(1.0f + __builtin_amdgcn_exp2f(-LOG2E * zq));
    const unsigned ob = (r >> 4) * 20480 + (r & 15) * 16, so = WS_O + (R2 * 8 + kq * 2) * 20480 + 5 * J2 * 256;
    stb16(rs, (u32x4){pk(mine[0] * rl, mine[1] * rl), pk(mine[2] * rl, mine[3] * rl), pk(mine[4] * rl, mine[5] * rl), pk(mine[6] * rl, mine[7] * rl)},
          ob + (3 * hd + h) * 256, so);
    if (h == 0) stb8(rs, (u32x2){pk(mine[8] * rl, mine[9] * rl), pk(mine[10] * rl, mine[11] * rl)}, ob + 512 + hd * 8, so);
  }
  STAMP(14);
  P3 y;
  y.ws = ws; y.lds = lds; y.w = w; y.l = l; y.tlo = tlo; y.thi = thi;
  y.wsrc = ws + WS_W3 + cb * 68 * 5120;
  y.voff = w * 5120 + l * 16;
  y.wa = lds + kq * 5120 + l * 16;
  y.f3off = WS_F3 + (R2 * 16 + J2) * 8;
  y.f3poll = WS_F3 + (rb >> 1) * 128;
  y.obase = WS_O + ((rb * 4 + hd * 2) * 20 + kq) * 1024;
  asm volatile("s_waitcnt lgkmcnt(0)\n\ts_barrier" : "+v"(a.hf[0][10]), "+v"(a.hf[1][10]), "+v"(a.hf[0][11]), "+v"(a.hf[1][11])::"memory");
  y.reads<0>();
  y.dma<2>(); y.dma<3>();
  y.run<0>(a.hf);
  STAMP(15);
  bar_lds();
#pragma unroll
  for (int t = 0; t < 2; ++t)
#pragma unroll
    for (int ft = 0; ft < 5; ++ft) *(LAS f32x4*)(size_t)(lds + ((hd * 10 + t * 5 + ft) * 4 + kq) * 1024 + l * 16) = y.acc[t][ft];
  bar_lds();
  STAMP(16);
  for (int tile = w; tile < 20; tile += 8) {
    const unsigned ad = lds + tile * 4096 + l * 16;
    const f32x4 v = (*(const LAS f32x4*)(size_t)ad + *(const LAS f32x4*)(size_t)(ad + 1024)) +
                    (*(const LAS f32x4*)(size_t)(ad + 2048) + *(const LAS f32x4*)(size_t)(ad + 3072));
    const int rtl = tile / 5, ft = tile - rtl * 5;
    const u32x2 o = {pk(v[0], v[1]), pk(v[2], v[3])};
    *(GAS u32x2*)(p.out + (size_t)(rb * 64 + rtl * 16 + i16) * 1280 + cb * 160 + ft * 32 + g * 8) = o;
  }
  if (b == 0 && w == 0 && l == 0) set_flag(ws, WS_E, tlo, thi);
  STAMP(17);
  STAMP(18);
  STAMP_OUT(0, 19);
}

extern "C" void kernel_launch(void* const* d_in, const int* in_sizes, int n_in, void* d_out, int out_size, void* d_ws, size_t ws_size,
                              hipStream_t stream) {
  Params p;
  p.x = (const char*)d_in[0]; p.wqkv = (const char*)d_in[1]; p.wo = (const char*)d_in[2];
  p.wg = (const char*)d_in[4]; p.wu = (const char*)d_in[5]; p.wd = (const char*)d_in[6]; p.gw = (const char*)d_in[7];
  p.out = (char*)d_out; p.ws = (char*)d_ws;
  (void)hipFuncSetAttribute((const void*)fwd, hipFuncAttributeMaxDynamicSharedMemorySize, LDS_BYTES);
  fwd<<<256, 512, LDS_BYTES, stream>>>(p);
}
```

```cpp
#include <hip/hip_runtime.h>
#include <stdint.h>

typedef __bf16 bf16x8 __attribute__((ext_vector_type(8)));
typedef __bf16 bf16x16 __attribute__((ext_vector_type(16)));
typedef __bf16 bf16x2 __attribute__((ext_vector_type(2)));
typedef short s16x4 __attribute__((ext_vector_type(4)));
typedef float f32x2 __attribute__((ext_vector_type(2)));
typedef float f32x4 __attribute__((ext_vector_type(4)));
typedef float f32x8 __attribute__((ext_vector_type(8)));
typedef float f32x16 __attribute__((ext_vector_type(16)));
typedef unsigned u32x2 __attribute__((ext_vector_type(2)));
typedef unsigned u32x4 __attribute__((ext_vector_type(4)));
typedef unsigned u32x16 __attribute__((ext_vector_type(16)));
typedef unsigned long long u64;
typedef __amdgpu_buffer_rsrc_t rsrc_t;
#define LAS __attribute__((address_space(3)))
#define GAS __attribute__((address_space(1)))
#define DI __device__ __forceinline__
#define SB() __builtin_amdgcn_sched_barrier(0)

#define PADKV 256
#define PADQ 256
constexpr unsigned KVP = 64 * 5120 + PADKV, QP = 64 * 2560 + PADQ;
#define KV0 0
constexpr unsigned WS_KV = KV0;
constexpr unsigned WS_Q = WS_KV + 16 * KVP;
constexpr unsigned WS_H = WS_Q + 16 * QP;
constexpr unsigned WS_O = WS_H + 128 * 48 * 1024;
constexpr unsigned WS_W3 = WS_O + 128 * 20 * 1024;
constexpr unsigned WS_G = WS_W3 + 8 * 36 * 5120;
constexpr unsigned WS_ONES = WS_G + 2048 * 4;
constexpr unsigned WS_E = WS_ONES + 1024;
constexpr unsigned WS_F1 = WS_E + 256;
constexpr unsigned WS_F2 = WS_F1 + 2048;
constexpr unsigned WS_F3 = WS_F2 + 2048;
constexpr unsigned WS_END = WS_F3 + 2048;
constexpr unsigned OOB = 0x7ffffff0u;
constexpr int AUX_SC1 = 16;
#define OUT_AUX 16
constexpr unsigned SPIN_CAP = 1u << 15;

struct Params {
  const char* x; const char* wqkv; const char* wo; const char* wg; const char* wu; const char* wd; const char* gw;
  char* out; char* ws;
};

struct Stamps {};
#define STAMP(i)
#define STAMP_OUT(ph, n)

DI int lane_id() { int l; asm volatile("v_mbcnt_lo_u32_b32 %0, -1, 0\n\tv_mbcnt_hi_u32_b32 %0, -1, %0" : "=v"(l)); return l; }
#define LANE_VARS const int l = lane_id(), i16 = l & 15, g = l >> 4, q = (l >> 2) & 3, pp = l & 3, r = l & 31, h = l >> 5; (void)i16; (void)g; (void)q; (void)pp; (void)r; (void)h
DI void glds16(const char* sbase, unsigned voff, unsigned lds_addr) {
  asm volatile("s_mov_b32 m0, %2\n\ts_nop 0\n\tglobal_load_lds_dwordx4 %0, %1" ::"v"(voff), "s"(sbase), "s"(lds_addr) : "memory");
}
template <int C> DI void set_m0(unsigned base) { asm volatile("s_add_i32 m0, %0, %1" ::"s"(base), "i"(C) : "scc", "memory"); }
template <int O> DI void glds16o(const char* sbase, unsigned voff) {
  asm volatile("global_load_lds_dwordx4 %0, %1 offset:%2" ::"v"(voff), "s"(sbase), "i"(O) : "memory");
}
template <int N> DI void wait_vm() { asm volatile("s_waitcnt vmcnt(%0)" ::"i"(N) : "memory"); }
template <int N> DI void wait_vm_bar() { asm volatile("s_waitcnt vmcnt(%0) lgkmcnt(0)\n\ts_barrier" ::"i"(N) : "memory"); }
DI void bar_lds() { asm volatile("s_waitcnt lgkmcnt(0)\n\ts_barrier" ::: "memory"); }

template <unsigned SO, unsigned SPAN> DI unsigned rebase(unsigned a) {
  if constexpr (SO + SPAN <= 65536) return a + SO;
  else { unsigned t = a + SO; asm volatile("" : "+v"(t)); return t; }
}
DI bf16x8 lds_b128(unsigned a) { return *(const LAS bf16x8*)(size_t)a; }
DI s16x4 lds_tr(unsigned a) { return __builtin_amdgcn_ds_read_tr16_b64_v4i16((LAS s16x4*)(size_t)a); }
DI bf16x8 lds_tr2(unsigned a0, unsigned a1) {
  s16x4 lo = lds_tr(a0), hi = lds_tr(a1);
  return __builtin_bit_cast(bf16x8, __builtin_shufflevector(lo, hi, 0, 1, 2, 3, 4, 5, 6, 7));
}
DI unsigned pk(float a, float b) { f32x2 v = {a, b}; return __builtin_bit_cast(unsigned, __builtin_convertvector(v, bf16x2)); }
DI float mul1(float a, float s) { return a * s; }
DI float mulv(float a, float b) { return a * b; }
DI float add1(float a) { return a + 1.0f; }
#define PS(x) asm volatile("" : "+s"(x))
#define PVV(x) asm volatile("" : "+v"(x))
DI unsigned here(unsigned v) { PS(v); return v; }
DI float upper(float x) { const unsigned u = __builtin_bit_cast(unsigned, x); const auto r = __builtin_amdgcn_permlane32_swap(u, u, false, false); const unsigned y = r[1]; return __builtin_bit_cast(float, y); }
DI int swap23(int r) { return (r & ~12) | ((r & 4) << 1) | ((r & 8) >> 1); }
DI u32x4 pair_rows(unsigned a0, unsigned a1, unsigned b0, unsigned b1) {
  const auto s0 = __builtin_amdgcn_permlane16_swap(a0, b0, false, false);
  const auto s1 = __builtin_amdgcn_permlane16_swap(a1, b1, false, false);
  return (u32x4){s0[0], s1[0], s0[1], s1[1]};
}
DI bf16x8 ldb16(rsrc_t rs, unsigned voff, unsigned soff) { return __builtin_bit_cast(bf16x8, __builtin_amdgcn_raw_buffer_load_b128(rs, voff, soff, AUX_SC1)); }
DI s16x4 ldb8(rsrc_t rs, unsigned voff, unsigned soff) { return __builtin_bit_cast(s16x4, __builtin_amdgcn_raw_buffer_load_b64(rs, voff, soff, AUX_SC1)); }
DI float ldb4(rsrc_t rs, unsigned voff, unsigned soff) { return __builtin_bit_cast(float, __builtin_amdgcn_raw_buffer_load_b32(rs, voff, soff, AUX_SC1)); }
DI void stb16(rsrc_t rs, u32x4 v, unsigned voff, unsigned soff) { __builtin_amdgcn_raw_buffer_store_b128(v, rs, voff + soff, 0, AUX_SC1); }
DI void stb8(rsrc_t rs, u32x2 v, unsigned voff, unsigned soff) { __builtin_amdgcn_raw_buffer_store_b64(v, rs, voff, soff, AUX_SC1); }
DI void stb4(rsrc_t rs, unsigned v, unsigned voff, unsigned soff) { __builtin_amdgcn_raw_buffer_store_b32(v, rs, voff, soff, AUX_SC1); }
DI u32x4 ld16_raw(const char* sbase, unsigned voff) { u32x4 v; asm volatile("global_load_dwordx4 %0, %1, %2 sc1" : "=v"(v) : "v"(voff), "s"(sbase) : "memory"); return v; }
template <int O> DI u32x4 ld16_tab(const char* sbase, unsigned voff) { u32x4 v; asm volatile("global_load_dwordx4 %0, %1, %2 offset:%3" : "=v"(v) : "v"(voff), "s"(sbase), "i"(O) : "memory"); return v; }
DI u32x2 ld8_raw(const char* sbase, unsigned voff) { u32x2 v; asm volatile("global_load_dwordx2 %0, %1, %2 sc1" : "=v"(v) : "v"(voff), "s"(sbase) : "memory"); return v; }
DI void set_flag(char* ws, unsigned off, unsigned tlo, unsigned thi) {
  __hip_atomic_store((GAS u64*)(ws + off), ((u64)thi << 32) | tlo, __ATOMIC_RELAXED, __HIP_MEMORY_SCOPE_AGENT);
}
#define MF32(a, b, c) __builtin_amdgcn_mfma_f32_32x32x16_bf16(a, b, c, 0, 0, 0)
#define MF16(a, b, c) __builtin_amdgcn_mfma_f32_16x16x32_bf16(a, b, c, 0, 0, 0)
#define MF8(a, b, c) __builtin_amdgcn_mfma_f32_32x32x8bf16_1k(a, b, c, 0, 0, 0)
DI f32x16 smf(bf16x8 a, f32x8 p, f32x16 c, int idx) {
  return __builtin_amdgcn_smfmac_f32_32x32x32_bf16(a, __builtin_bit_cast(bf16x16, p), c, idx, 0, 0);
}
#define LO8(s) __builtin_shufflevector(s, s, 0, 1, 2, 3, 4, 5, 6, 7)
#define HI8(s) __builtin_shufflevector(s, s, 8, 9, 10, 11, 12, 13, 14, 15)

constexpr float LOG2E = 1.4426950408889634f;
constexpr float QSCALE = 0.22360679774997896f * LOG2E;
constexpr unsigned LDS_BYTES = 160 * 1024, lds = 0;
constexpr unsigned L_REL = 147456;
constexpr unsigned L_JUNK = 159744;
constexpr unsigned L_BLK = 20480;
constexpr unsigned L_CNT = LDS_BYTES - 16;
constexpr unsigned L_CN2 = 9216;
constexpr unsigned slot_of(int s) { return s < 10 ? ((9 - s) % 3) * 32768 : (5 - (s - 10) % 6) * 24576; }

DI void wait8(u64 f, const char* base, unsigned voff, u64 tag) {
  unsigned n;
  asm volatile("s_mov_b32 %1, %5\n1:\n\tv_cmp_ne_u64 vcc, %4, %0\n\ts_cmp_eq_u64 vcc, 0\n\ts_cbranch_scc1 2f\n\t"
               "s_sleep 2\n\tglobal_load_dwordx2 %0, %2, %3 sc1\n\ts_waitcnt vmcnt(0)\n\t"
               "s_sub_u32 %1, %1, 1\n\ts_cbranch_scc0 1b\n2:"
               : "+v"(f), "=&s"(n) : "v"(voff), "s"(base), "s"(tag), "i"(SPIN_CAP) : "vcc", "scc", "memory");
}

constexpr int g_stage(int gi) { return gi < 40 ? gi >> 2 : gi < 88 ? 10 + (gi - 40) / 6 : 18 + ((gi - 88) & 3) / 2; }
constexpr int g_ks(int gi) { return gi < 40 ? (gi >> 1) & 1 : gi < 46 ? gi & 1 : gi < 88 ? ((gi - 40) % 6) / 3 : gi & 1; }
constexpr int g_t(int gi) { return gi < 46 ? (gi - 40) / 2 : gi < 88 ? (gi - 40) % 3 : (gi - 88) / 4; }
constexpr bool first_of(int gi) { return gi < 40 ? (gi & 3) == 0 : gi <= 88 && (gi - 40) % 6 == 0; }
constexpr int pstage(int p) { return p < 40 ? p >> 2 : 10 + (p - 40) / 3; }
constexpr int pend(int s) { return s < 10 ? 4 * (s + 1) : 40 + 3 * (s - 9); }
constexpr int bar_at(int s) { return s < 10 ? 4 * s - 2 : 38 + 6 * (s - 10); }
constexpr bool strict(int s) { return s < 8 || s == 9; }
constexpr int free_at(int T) {
  if (T < 3 || T == 10 || T == 11) return -9;
  const int u = T < 10 ? T - 3 : T < 15 ? T - 5 : T - 6;
  return bar_at(strict(u + 1) ? u + 1 : u + 2);
}
#define P_TA 6
#define P_TG 14, 56
#define P_NG 3
#define FULLT 0
#define P_GU 30, 30, 31, 32, 33, 33, 34, 35, 36, 37, 38, 39
#define P_ST 40, 41, 42, 43
#define P_RD 70
#define P_LT 56, 65, 76
#define P_BLT 15
#define P_LATE 5
#define P_EPOS 1
#define P_POLL 81
#define P_F1 14
#define EARLY1 0
#define TPOS 0
#define TS0 1
#define TS1 6
constexpr bool T_AFTER1 = TPOS == 2 || (TPOS == 1 && EARLY1 == 1);
constexpr int G_TA = P_TA, G_TG[2] = {P_TG}, NG = P_NG;
constexpr int cold_at(int g) { return (g == G_TA) + (g == G_TG[0]) + (g == G_TG[1]); }
constexpr int N0 = 8, NS = 52, G_GU[12] = {P_GU}, G_ST[4] = {P_ST}, B_F1 = P_F1, G_RD = P_RD, G_POLL = P_POLL, G_ATT = 86, G_LT[3] = {P_LT}, B_LT = P_BLT;
constexpr int piece_at(int p, int late = 0) { return p < N0 ? -1 : p < 12 ? (p - N0) / 2 : p < 40 ? p - 10 : p < NS ? G_GU[p - 40] : bar_at(pstage(p) - 3) + late * P_LATE; }
constexpr int nx(int g) {
  return cold_at(g) + (g == G_LT[0]) + (g == G_LT[1]) + (g == G_LT[2]) + (g == G_ST[0] || g == G_ST[1] || g == G_ST[2] || g == G_ST[3] || g == bar_at(B_F1) || g == G_RD || g == G_RD + 1 || g == G_POLL);
}
constexpr bool m0_new(int p) {
  bool n = p < 40 ? (p & 1) == 0 : (p - 40) % 3 == 0;
  for (int g = piece_at(p > 0 ? p - 1 : 0) + 1; g <= piece_at(p); ++g) n = n || cold_at(g) || g == G_RD || g == G_RD + 1;
  return n;
}
struct Plan {
  int cum[101], wait[18];
  bool ok;
  constexpr int younger(int j, int g) const { int n = 0; for (int u = j + 1; u < g; ++u) n += nx(u); return n; }
  constexpr Plan(int late) : cum(), wait(), ok(true) {
    for (int g = 0; g <= 100; ++g) { int c = 0; while (c < 70 && piece_at(c, late) < g) ++c; cum[g] = c; }
    for (int p = 1; p < 70; ++p) ok = ok && piece_at(p, late) >= piece_at(p - 1, late) && piece_at(p, late) >= free_at(pstage(p));
    for (int s = 0; s < 18; ++s) {
      const int g = s ? bar_at(s) : 0, need = pend(s);
      int n = cum[g] - need + younger(piece_at(need - 1, late), g) + (TPOS != 3 && TPOS != -1 && (s == 0 || (s == 1 && T_AFTER1))) + (s == 0 || (s == 1 && EARLY1 != 0));
      const int x = s == B_F1 ? G_ST[3] : -1;
      if (x >= 0) { const int m = cum[g] - cum[x] + younger(x, g); n = m < n ? m : n; ok = ok && x < g; }
      if (s == B_LT) { const int y = G_LT[0], m = cum[g] - cum[y] + younger(y, g) + nx(y) - 1; n = m < n ? m : n; }
      ok = ok && cum[g] >= need && n < 63;
      wait[s] = n;
    }
    ok = ok && cum[bar_at(18)] == 70 && G_RD + 1 < bar_at(18) && G_RD >= bar_at(B_LT);
    for (int p = 9; p < NS; ++p) ok = ok && !(m0_new(p) && piece_at(p) == piece_at(p - 1));
  }
};
constexpr Plan PL{0}, PL1{1};
static_assert(PL.ok && PL1.ok && PL.cum[40] == NS, "schedule");
constexpr int vm_at(int s) { return PL.wait[s] < PL1.wait[s] ? PL.wait[s] : PL1.wait[s]; }

#define P_GC 0x72dcba98f654e310ull
constexpr unsigned gcol(unsigned J) { return (unsigned)(P_GC >> (4 * J)) & 15; }
struct Rows {
  unsigned e[64][32];
  constexpr Rows(unsigned b0) : e() {
    for (unsigned i = 0; i < 64; ++i) {
      const unsigned b = b0 + (i >> 3), v = i & 7, v3 = v & 3, v2 = v >> 2, v1 = v >> 1, hd = v & 1;
      const unsigned x1 = b & 1, x2 = (b >> 1) & 3, i7 = (b >> 3) & 7, i2 = b >> 6, R = 8 * x1 + i7, J = 4 * x2 + i2, J2 = 2 * (b & 7) + (b >> 7);
      const unsigned u0 = 2 * b + v2, u = u0 < 360 ? u0 : 359, ks = u / 10, cu = u % 10, ft = 4 * cu + v3;
      const unsigned rot = (b >> 3) & 15, rb = 8 * x2 + i7, cb = 4 * x1 + i2, tb = WS_KV + J2 * KVP + v1 * (16 * 5120);
      unsigned* o = e[i];
      o[0] = ((ks < 16 ? ks + 32 : ks - 16) * 320 + v3 * 80 + cu) << 7;
      o[1] = u0 < 360 ? (ft / 5 * 180 + ks * 5 + (ft + 5 - (ft / 5 & 1)) % 5) * 1024 + WS_W3 : OOB;
      o[2] = (b * 8 + v) * 640;
      o[3] = WS_F2 + (R * 16 + J) * 8;
      o[4] = WS_H + ((R * 4 + v3) * 96 + 3 * gcol(J)) * 1024 + v2 * 512;
      o[5] = rot; o[6] = tb;
      o[7] = WS_O + (rot * 8 + v) * 20480 + J2 * 1280;
      o[8] = tb + rot * 5120;
      o[9] = WS_G + rot * 512 + v1 * 128;
      o[10] = rb; o[11] = cb;
      o[12] = v * (32 * 1280) + cb * 160;
      o[13] = WS_W3 + cb * (36 * 5120) + v * 5120;
      o[14] = WS_W3 + cb * (36 * 5120) + i7 * (180 * 128);
      o[15] = WS_H + 24 * 1024 + (rb * 4 + i2) * 49152 + v * 3072;
      for (unsigned k = 0; k < 4; ++k) {
        const unsigned t = (v + 3 + v2 * 2 + k) & 3;
        o[16 + k] = WS_Q + J2 * QP + rot * (4 * 2560) + ((v1 + k + 1) & 3) * 2560;
        o[20 + k] = WS_H + (rb * 192 + v) * 1024 + t * 49152;
        o[24 + k] = WS_O + (rb * 80 + v) * 1024 + t * 20480;
      }
      o[28] = hd;
    }
  }
};
template <int K> constexpr Rows ROWS{8 * K};
#define R4(k) ROWS<k>, ROWS<k + 1>, ROWS<k + 2>, ROWS<k + 3>
alignas(128) constexpr Rows TAB[32] = {R4(0), R4(4), R4(8), R4(12), R4(16), R4(20), R4(24), R4(28)};

struct Lanes {
  unsigned e[8][5][64][4];
  constexpr Lanes() : e() {
    for (unsigned v = 0; v < 8; ++v)
      for (unsigned l = 0; l < 64; ++l) {
        const unsigned hd = v & 1, r = l & 31, h = l >> 5, l16 = l * 16, k = ((l16 & 64) << 2) | ((l16 >> 1) & 192) | (l16 & 0x230);
        unsigned* o = e[v][0][l];
        o[0] = l16; o[1] = (l >> 3) * 1280 + (l & 7) * 16; o[2] = r * 8; o[3] = (l < 4 ? l : 4) * 128;
        o = e[v][1][l];
        o[0] = l16 + hd * 1536; o[1] = h ? OOB : l16 + hd * 8 + 1024; o[2] = k + hd * 1536; o[3] = h ? OOB : k + hd * 8 + 1024;
        o = e[v][2][l];
        o[0] = r < 20 ? (r >> 2) * 256 + (r & 3) * 32 + h * 16 + 2560 + hd * 1280 : r == 20 ? WS_ONES : OOB;
        o[1] = r < 20;
        o[2] = (l & 15) * 16 + h * 256 + ((l >> 4) & 1) * 49152;
        o[3] = lds + L_REL + (v >> 2) * 4096 + (v & 3) * 32 + (l >> 4) * 1024 + ((l >> 2) & 3) * 128 + (l & 3) * 8;
        o = e[v][3][l];
        const unsigned ob = (l16 & 256) * 80 + (l16 & 255);
        o[0] = lds + v * L_BLK + l16; o[1] = lds + v * L_BLK + (8 * (l >> 4) + ((l >> 2) & 3)) * 160 + (l & 3) * 8;
        o[2] = ob + h * 256 + hd * 768; o[3] = h ? OOB : ob + 512 + hd * 8;
        o = e[v][4][l];
        for (unsigned d = 1; d < 4; ++d) o[d - 1] = l16 + lds + ((v + 2 * d) & 7) * L_BLK + (3 - d) * 3072;
        o[3] = (256 + l) / 10 * (1280 - 160) + l16 + 4096;
      }
  }
};
alignas(128) constexpr Lanes LTAB{};

struct Attn;
struct P1 {
  const char* xb; const char* wqb; const char* gub; const char* tb1; const char* tb2; const char* rel0; const char* tb3; char* ws;
  rsrc_t rs;
  unsigned xo0, xo1, wo0, wo1, go0, go1, go2, to1, to2, w, w2k, wst, rt;
  unsigned xa[2][2], wa[2][4], ga, gdst;
  unsigned sta[4], f1off, hsa, hso, b, t0, t1, t2, t3, relo, pvo;
  float sc, carry;
  u64 epoch, tag, pf;
  u32x16 ta, tc;
  u32x4 lt[3];
  const char* ltb;
  bf16x8 xf[40];
  s16x4 wh[3][4];
  f32x4 acc[4][2], gacc[6][2];
  u32x4 pq[4];
  f32x2 cv[4];
  float he[2], hp[2]; unsigned hk[4];

  DI void touch(const char* base, unsigned voff) const {
    asm volatile("s_mov_b32 m0, %2\n\ts_nop 0\n\tglobal_load_lds_dword %0, %1" ::"v"(voff), "s"(base), "s"(lds + L_JUNK + w * 256) : "memory");
  }
  template <int P> DI void piece_m0() const {
    constexpr int T = pstage(P);
    if constexpr (T < 10) set_m0<(int)slot_of(T) + ((P & 2) ? 0 : 16384 - T * 128)>(w2k);
    else set_m0<(int)slot_of(T)>(gdst);
  }
  template <int P> DI void piece_ld() const {
    constexpr int T = pstage(P);
    if constexpr (T < 10) {
      constexpr int i = P & 3;
      if constexpr (i < 2) glds16o<T * 128 + i * 1024>(xb, i ? xo1 : xo0);
      else glds16o<(i & 1) * 1024>(wqb, i & 1 ? wo1 : wo0);
    } else {
      constexpr int i = (P - 40) % 3;
      glds16o<i * 1024>(gub, i == 0 ? go0 : i == 1 ? go1 : go2);
    }
  }
  template <int P> DI void step() {
    if constexpr (P > 4 && P < 40 && (P & 3) == 2) { wqb += wst; PS(wqb); }
    if constexpr (P > 40 && P < 70 && (P - 40) % 3 == 0) { gub += 64 * 3072; PS(gub); }
  }
  template <int A, int B> DI void loads() const { if constexpr (A < B) { piece_ld<A>(); loads<A + 1, B>(); } }
  template <int A> DI void pair() { step<A>(); piece_m0<A>(); asm volatile("s_nop 0"); loads<A, A + 2>(); }
  template <int G> DI void ahead() {
    constexpr int P = PL.cum[G];
    if constexpr (G < 40 && PL.cum[G + 1] > P) {
      if constexpr (m0_new(P) && !cold_at(G)) piece_m0<P>();
      if constexpr (G < 39 && (P >= 40 || G < 2)) step<P>();
    }
  }
  template <int T, int LATE> DI void trio() const {
    asm volatile("s_bitcmp%c7_b32 %0, 2\n\ts_cbranch_scc1 1f\n\ts_add_i32 m0, %1, %6\n\ts_nop 0\n\tglobal_load_lds_dwordx4 %3, %2\n\t"
                 "global_load_lds_dwordx4 %4, %2 offset:1024\n\tglobal_load_lds_dwordx4 %5, %2 offset:2048\n1:"
                 ::"s"(w), "s"(gdst), "s"(gub), "v"(go0), "v"(go1), "v"(go2), "i"(slot_of(T)), "i"(LATE) : "scc", "memory");
  }
  static constexpr int wcnt(int vm, int lgkm) { return (vm & 15) | ((vm >> 4) << 14) | 0x70 | (lgkm << 8); }
  static constexpr int nread(int mode, int g) { return g >= 100 ? 0 : g >= 40 ? 4 : (g & 1) ? (mode == 2 ? 0 : 4) : (mode == 2 ? 4 : 6); }
  template <int MODE, int S> DI void sync() {
    if constexpr (S == 0) { asm volatile("s_waitcnt vmcnt(%2) lgkmcnt(0)\n\ts_barrier\n\ts_mov_b64 %0, %1" : "=s"(tag) : "s"(epoch), "i"(PL.wait[0]) : "memory"); tag += 0x9E3779B97F4A7C15ull; }
    else if constexpr (S < 18) {
      __builtin_amdgcn_s_waitcnt(wcnt(vm_at(S), strict(S) ? 0 : nread(MODE, bar_at(S) + 1)));
      SB();
      if constexpr (S == B_LT) asm volatile("s_barrier" : "+v"(lt[0])::"memory"); else asm volatile("s_barrier" ::: "memory");
    } else {
      asm volatile("s_waitcnt vmcnt(0)" : "+v"(pf), "+v"(lt[1]), "+v"(lt[2])::"memory");
      wait8(pf, ws + WS_F1 + w * 256, lt[0][2], tag);
      __builtin_amdgcn_s_waitcnt(wcnt(63, 4));
      SB();
      asm volatile("s_barrier" ::: "memory");
    }
  }
  template <int MODE, int GI, int K> DI void read1() {
    constexpr unsigned so = slot_of(g_stage(GI));
    constexpr int ks = g_ks(GI);
    if constexpr (GI < 40) {
      constexpr int hf = GI & 1, hi = so >> 16;
      constexpr unsigned sl = so & 65535;
      if constexpr (K >= 4) xf[(GI >> 1) * 2 + K - 4] = lds_b128(xa[hi][ks] + sl + (K - 4) * 2048);
      else if constexpr (MODE == 2) { if constexpr (hf == 0 && K < 2) wh[GI % 3][K] = *(const LAS s16x4*)(size_t)(wa[hi][0] + sl + ks * 64 + K * 8); }
      else wh[GI % 3][K] = lds_tr(wa[hi][2 * hf + (K >> 1)] + sl + ks * 8192 + (K & 1) * 1024);
    } else {
      if constexpr (K == 0 && (GI == 52 || GI == 64 || GI == 76 || GI == 88)) { ga += GI == 76 ? 98304 : -49152; asm volatile("" : "+v"(ga)); }
      wh[GI % 3][K] = lds_tr(ga + so % 49152 + ks * 6144 + g_t(GI) * 64 + (K >> 1) * 12288 + (K & 1) * 768);
    }
  }
  template <int KK> static DI void chain(f32x4& c, bf16x8 a, bf16x8 b) {
    if constexpr (KK == 0) c = MF16(a, b, ((f32x4){0.f, 0.f, 0.f, 0.f}));
    else if constexpr (KK == 19) c = MF16(a, b, c);
    else asm volatile("v_mfma_f32_16x16x32_bf16 %0, %1, %2, %0" : "+v"(c) : "v"(a), "v"(b));
  }
  template <int MODE, int GI, int I> DI void mfma1() {
    constexpr int n = I >> 1, m = I & 1;
    const bf16x8 a = __builtin_bit_cast(bf16x8, __builtin_shufflevector(wh[GI % 3][2 * n], wh[GI % 3][2 * n + 1], 0, 1, 2, 3, 4, 5, 6, 7));
    if constexpr (GI < 40) {
      constexpr int hf = GI & 1, kk = GI >> 1;
      if constexpr (MODE != 2 || (hf == 0 && n == 0)) {
        if constexpr (MODE == 1) chain<kk>(acc[2 * hf + n][m], xf[kk * 2 + m], a); else chain<kk>(acc[2 * hf + n][m], a, xf[kk * 2 + m]);
      }
    } else {
      constexpr int kk = (g_stage(GI) - 10) * 2 + g_ks(GI), t = g_t(GI);
      chain<kk>(gacc[3 * n + t][m], a, xf[kk * 2 + m]);
    }
  }
  template <int T, int J> DI void silu_a() {
#pragma unroll
    for (int e = 0; e < 2; ++e) {
      const float gt = gacc[T][J >> 1][(J & 1) * 2 + e], up = gacc[3 + T][J >> 1][(J & 1) * 2 + e];
      he[e] = __builtin_amdgcn_exp2f(mul1(gt, -LOG2E));
      hp[e] = mulv(gt, up);
    }
  }
  template <int J> DI void silu_b() { hk[J] = pk(mulv(hp[0], __builtin_amdgcn_rcpf(add1(he[0]))), mulv(hp[1], __builtin_amdgcn_rcpf(add1(he[1])))); }
  template <int T> DI void h_store() { stb16(rs, pair_rows(hk[0], hk[1], hk[2], hk[3]), hsa + T * 1024, 0); }
  template <int GI, int I> DI void extra(Attn& a);
  template <int MODE, int GI, int I> DI void valu(Attn& a) {
    if constexpr (MODE == 0 && GI >= 40 && GI < 44 && I == 0) { constexpr int n = GI - 40; pq[n] = pair_rows(pq[n][0], pq[n][1], pq[n][2], pq[n][3]); }
    if constexpr (GI >= 39 && GI < 43) {
      constexpr int n = GI - 39;
      if constexpr (MODE == 0) {
        auto mul = [&](int u) { cv[u] = (f32x2){acc[n][u >> 1][(u & 1) * 2], acc[n][u >> 1][(u & 1) * 2 + 1]} * sc; PVV(cv[u]); };
        auto cvt = [&](int u) { unsigned x = pk(cv[u][0], cv[u][1]); PVV(x); pq[n][u] = x; };
        if constexpr (I == 0) mul(0);
        if constexpr (I == 1) { cvt(0); mul(1); }
        if constexpr (I == 2) { cvt(1); mul(2); mul(3); }
        if constexpr (I == 3) { cvt(2); cvt(3); }
      } else if constexpr (MODE == 1) {
        pq[n][I] = pk(acc[n][I >> 1][(I & 1) * 2], acc[n][I >> 1][(I & 1) * 2 + 1]);
      } else if constexpr (I == 1) {
        pq[n] = (u32x4){0x3f803f80u, 0x3f803f80u, 0x3f803f80u, 0x3f803f80u};
        if constexpr (n == 0) pq[0][0] = __builtin_bit_cast(unsigned, (lane_id() & 16) ? acc[0][1][0] : acc[0][0][0]);
      }
    }
    extra<GI, I>(a);
    if constexpr (GI >= 92 && I == 1) silu_a<(GI - 92) / 4, GI & 3>();
    if constexpr (GI >= 92 && I == 3) silu_b<GI & 3>();
  }
  template <int MODE, int GI> DI void vmem(Attn& a);
  template <int MODE, int GI, int END> DI void run(Attn& a) {
    if constexpr (GI < END) {
      constexpr int NX = GI + 2;
      constexpr bool RD = NX < 100, X = NX < 40 && (NX & 1) == 0, LAST = RD && !X && first_of(NX + 1) && strict(g_stage(NX + 1));
      if constexpr (RD && first_of(NX)) sync<MODE, g_stage(NX)>();
      else if constexpr (GI > 0 && !(first_of(NX - 1) && strict(g_stage(NX - 1)) && GI > 1)) __builtin_amdgcn_s_waitcnt(wcnt(63, nread(MODE, GI + 1)));
      if constexpr (GI == G_LT[0]) lt[0] = ld16_tab<0>(ltb, t3);
      if constexpr (GI == G_TA) touch(tb1, to1);
      if constexpr (GI == G_TG[0] || GI == G_TG[1]) touch(tb2, to2);
      if constexpr (GI < 40 && cold_at(GI) && PL.cum[GI + 1] > PL.cum[GI]) piece_m0<PL.cum[GI]>();
      constexpr bool E3 = GI >= 40 && PL.cum[GI + 1] > PL.cum[GI];
      if constexpr (E3) step<PL.cum[GI]>();
      if constexpr (E3 && P_EPOS == 0) trio<pstage(PL.cum[GI]), 0>();
      SB(); mfma1<MODE, GI, 0>(); SB();
      if constexpr (RD) { if constexpr (X) read1<MODE, NX, 4>(); read1<MODE, NX, 0>(); if constexpr (LAST) read1<MODE, NX, 1>(); }
      if constexpr (E3 && P_EPOS == 1) trio<pstage(PL.cum[GI]), 0>();
      if constexpr (GI > 1 && GI < 29 && (GI & 3) == 3) step<GI + 11>();
      valu<MODE, GI, 0>(a);
      SB(); mfma1<MODE, GI, 1>(); SB();
      if constexpr (RD) { if constexpr (LAST) { read1<MODE, NX, 2>(); read1<MODE, NX, 3>(); } else read1<MODE, NX, 1>(); if constexpr (X) read1<MODE, NX, 5>(); }
      if constexpr (!X && !LAST) vmem<MODE, GI>(a);
      valu<MODE, GI, 1>(a);
      SB(); mfma1<MODE, GI, 2>(); SB();
      if constexpr (RD && !LAST) read1<MODE, NX, 2>();
      if constexpr (X || LAST) vmem<MODE, GI>(a); else ahead<GI + 1>();
      valu<MODE, GI, 2>(a);
      SB(); mfma1<MODE, GI, 3>(); SB();
      if constexpr (RD && !LAST) read1<MODE, NX, 3>();
      if constexpr (X || LAST) ahead<GI + 1>();
      valu<MODE, GI, 3>(a);
      SB();
      if constexpr (GI >= 40 && PL1.cum[GI + 1] > PL1.cum[GI]) trio<pstage(PL1.cum[GI]), 1>();
      run<MODE, GI + 1, END>(a);
    }
  }
  template <int MODE, int GI> DI void reads_all() {
    if constexpr ((GI & 1) == 0) read1<MODE, GI, 4>();
    read1<MODE, GI, 0>(); read1<MODE, GI, 1>();
    if constexpr ((GI & 1) == 0) read1<MODE, GI, 5>();
    read1<MODE, GI, 2>(); read1<MODE, GI, 3>();
  }
  DI void head(Stamps& st, Attn& a, Params& p);
  template <int MODE> DI void qkv(Stamps& st, Attn& a, unsigned J) {
    const unsigned t = threadIdx.x;
    if constexpr (MODE == 0) {
      const unsigned jj = J < 5 ? J : J - 5, k = (J < 5 ? QP : KVP) - 2560;
      const unsigned u = ((t >> 5) & 9) + 16 * jj, t0 = ((t << 4) & 0x13ff) + here(rt * (J < 5 ? 2560 : 5120) + jj * 8192 + (J < 5 ? WS_Q : WS_KV));
      sc = J < 5 ? QSCALE : 1.0f;
#pragma unroll
      for (int n = 0; n < 4; ++n) sta[n] = __umul24((__umul24(u, 205) + 2 * n * 205) >> 10, k) + t0;
    } else if constexpr (MODE == 1) {
      const unsigned u = ((t >> 2) & 3) + here(32 * (J - 10) + (w & 4) * 4);
      const unsigned t0 = ((u << 8) | ((t & 3) << 5) | ((t & 32) << 2) | (t & 16)) + here(WS_KV + 2560 - 512 + rt * 5120);
#pragma unroll
      for (int n = 0; n < 4; ++n) {
        const unsigned pr = (__umul24(u, 205) + 4 * n * 205) >> 11;
        const unsigned e = __builtin_amdgcn_ubfe(01021622132u, __mul24(pr, -30) + (__umul24(u, 3) + 12 * n), 3);
        sta[n] = (e << 8) + (__umul24(pr, KVP - 2560) + t0);
      }
    } else {
      wa[0][0] = t & 48; wa[1][0] = wa[0][0] + 65536;
      sta[0] = (t & 31) * 4 + here(WS_G + rt * 128) + ((t & 0x120) << 22);
      sta[1] = w == 4 ? WS_ONES - 1024 + (t & 63) * 16 : OOB;
      sta[2] = OOB; sta[3] = OOB;
      PVV(wa[0][0]); PVV(wa[1][0]);
    }
    asm volatile("" : "+v"(sta[0]), "+v"(sta[1]), "+v"(sta[2]), "+v"(sta[3]));
    STAMP(4);
    sync<MODE, 0>();
    STAMP(5);
    reads_all<MODE, 0>(); SB(); reads_all<MODE, 1>();
    run<MODE, 0, 44>(a);
    STAMP(6);
    run<MODE, 44, 56>(a);
    carry = __builtin_bit_cast(float, ga);
    PVV(carry);
  }
  DI void join(Attn& a, const Params& p);
};

#define P_NH 13
#define P_PUB 8
#define P_TW 14
#define P_TH 40
#define P_DS 0, 2, 4, 7, 11, 11
#define P_NPRE 2
#define P_L3 60
#define P_L4 63
#define P_SW 52
constexpr int NH = P_NH;
constexpr int T_PUB = P_PUB, T_PUB2 = T_PUB + 12, T_W = P_TW, T_H = P_TH, T_F3 = 54, NPRE = P_NPRE;
constexpr int ND = 24 - NH + 35, D_SLOT[6] = {P_DS};
static_assert(D_SLOT[5] == 24 - NH && T_PUB % 4 == 0 && T_W < T_PUB2, "order");
#define P_TS 6
#define P_OW 12
#define P_DC 8
#define P_NB 55
#define P_LR 20
#define P_GAP 3
#define P_SL 4
#define P_IL 2
#define P_LAG 7
constexpr int TS = P_TS, NM = 170;
constexpr int d_item(int n) {
  if (n < 24 - NH) return NH + n;
  n -= 24 - NH;
  if (n < 5 * (TS - 4)) return 120 + n;
  n -= 5 * (TS - 4);
  if (n >= P_OW && n < P_OW + 10) return 24 + n - P_OW;
  if (n >= P_OW) n -= 10;
  return 100 + 5 * (TS + n % (9 - TS)) + n / (9 - TS);
}
struct DPlan {
  int j[NM], i[NM], n[NM + 1], out[NM + 1];
  bool ok;
  constexpr DPlan() : j(), i(), n(), out(), ok(true) {
    int u = 0, done[20] = {}, lastw[45] = {}, lasta[34] = {}, cap[NM] = {};
    for (; u < 20 * TS; ++u) { j[u] = u / 20; i[u] = u % 20; }
    for (int t = 0; t < 20; t += P_IL)
      for (int k = TS; k < 9; ++k)
        for (int e = t; e < t + P_IL; ++e) if (k < 8 || (e & 3) < 2) { j[u] = k; i[u] = e; done[e] = u++; }
    for (u = 0; u < NM; ++u) { lastw[5 * j[u] + (i[u] >> 2)] = u; lasta[4 * j[u] + (i[u] & 3)] = u; }
    int freed = 0;
    for (u = 0; u < NM; ++u) {
      freed += (lastw[5 * j[u] + (i[u] >> 2)] == u) + (lasta[4 * j[u] + (i[u] & 3)] == u);
      const int rd = u < P_LR ? 0 : (u - P_LR) / 4 + 1;
      cap[u] = P_NB - (NH + 10 + (rd < 10 ? rd : 10) + (u < 19 ? u + 2 : 20)) + freed;
    }
    for (u = NM - 2; u >= 0; --u) cap[u] = cap[u] < cap[u + 1] ? cap[u] : cap[u + 1];
    n[0] = D_SLOT[5] + P_DC;
    int last = -9;
    for (u = 0; u < NM; ++u) {
      n[u + 1] = n[u]; out[u + 1] = out[u];
      if (n[u] < ND && n[u] < cap[u] && u - last >= P_GAP) { ++n[u + 1]; last = u; }
      if (u >= done[out[u]] + P_LAG) ++out[u + 1];
    }
    for (u = 0; u < NM; ++u)
      for (int k = n[u]; k < ND; ++k) ok = ok && d_item(k) != 100 + 5 * j[u] + (i[u] >> 2) && d_item(k) != 4 * j[u] + (i[u] & 3) && (u < 20 * TS || d_item(k) >= 100);
  }
};
constexpr DPlan DP{};
static_assert(DP.ok && P_DC == 8 && TS == 6, "a load behind its use");
struct Attn {
  f32x16 s[3], o[4];
  bf16x8 q16[4], k16[16], v0[16], v1[16];
  s16x4 q4[4], k4[16], nil;
  bf16x8 A[34], W[45];
  f32x4 acc[4][5];
  rsrc_t rs, rw;
  char* ws; const char* wd; const char* ltb; const char* e0; const char* e1;
  unsigned ko, k4o, vo, vsel, qo, q4o;
  unsigned tb, rot, to0, qs[4], rb, cb, rela, lpo, ra[3], so16, so8, hb[4], obs[4];
  unsigned l16, w, hd;
  unsigned tlo, thi, f2off, hbase;
  unsigned wv[5], wra, wr4, tj0, tj1, wdoff, reldst, oclr, w3off, obase, scr, w4, zoff, wtouch, htouch;
  float zq;
  u64 pf;
  bf16x8 relv;
  int idxv;
  DI unsigned toff(int i) const { unsigned r; asm("s_mul_i32 %0, %1, 5120" : "=s"(r) : "s"((rot + i) & 15)); return r + tb; }
  template <int I> DI void loadk() { const unsigned so = toff(I); k16[I] = ldb16(rs, ko, so); k4[I] = ldb8(rs, k4o, so); }
  template <int I> DI void loadv() { const unsigned va = __umul24(vsel, toff(I)) + vo; v0[I] = ldb16(rs, va, 0); v1[I] = ldb16(rs, va + 128, 0); }
  template <int N> DI void first() {
    if constexpr (N == 0) { k16[0] = ldb16(rs, ko, to0); k4[0] = ldb8(rs, k4o, to0); }
    else if constexpr (N < 5) { q16[N - 1] = ldb16(rs, qo, qs[N - 1]); q4[N - 1] = ldb8(rs, q4o, qs[N - 1]); }
    else if constexpr (N < 4 + 2 * NPRE) { if constexpr (N & 1) loadv<(N - 5) / 2>(); else loadk<(N - 4) / 2>(); }
  }
  template <int B, int E> DI void exp4() {
#pragma unroll
    for (int i = E; i < E + 4; ++i) s[B][i] = __builtin_amdgcn_exp2f(s[B][i]);
  }
  template <int N> DI unsigned oaddr() const { return obs[N & 3] + ((N >> 2) - 6) * 8192 - (N < 32 ? 0 : w4 * 1024); }
  template <int N> DI void loada() {
    if constexpr (N < 24) A[N] = ldb16(rs, l16, hb[N & 3] + (N >> 2) * 8192);
    else A[N] = ldb16(rs, l16, oaddr<N>());
  }
  template <int N> DI void loadw() { W[N] = ldb16(rs, l16, w3off + (N / 5 - 4) * 40960 + (N % 5) * 1024 - (N < 40 ? 0 : w4 * 5120)); }
  template <int N, int E> DI void direct() {
    if constexpr (N < E && N < ND) { if constexpr (d_item(N) < 100) loada<d_item(N)>(); else loadw<d_item(N) - 100>(); direct<N + 1, E>(); }
  }
  template <int N> DI void readw() { const unsigned ad = (N % 5 == 4 ? wr4 : wra + (N % 5) * 32) + (N / 5) * 5120; W[N] = lds_tr2(ad, ad + 640); }
  template <int N, int C> DI void publish(unsigned foff) const {
    unsigned t0 = here(1u); u64 sv;
    asm volatile("s_waitcnt vmcnt(%6)\n\ts_mov_b64 %1, exec\n\ts_mov_b64 exec, 1\n\tds_add_rtn_u32 %0, %2, %0 offset:%7\n\ts_waitcnt lgkmcnt(0)\n\t"
                 "v_cmp_eq_u32 vcc, 7, %0\n\ts_and_b64 exec, exec, vcc\n\ts_cbranch_execz 1f\n\tglobal_store_dwordx2 %3, %4, %5 sc1\n1:\n\ts_mov_b64 exec, %1"
                 : "+&v"(t0), "=&s"(sv) : "v"(lds + L_CNT), "v"(foff), "v"(((u64)thi << 32) | tlo), "s"(ws), "i"(N), "i"(4 * C) : "vcc", "scc", "memory");
  }
  DI void sign() const { asm volatile("ds_add_u32 %0, %1 offset:8" ::"v"(here(lds + L_CNT)), "v"(here(1u)) : "memory"); }
  template <int N> DI void joined(unsigned skip, unsigned ad, unsigned t0) const {
    unsigned n;
    asm volatile("s_cmp_lg_u32 %5, 0\n\ts_cbranch_scc1 2f\n\ts_mov_b32 %1, %3\n1:\n\tds_read_b32 %0, %2 offset:8\n\ts_waitcnt lgkmcnt(0)\n\tv_cmp_gt_u32 vcc, %4, %0\n\ts_cmp_eq_u64 vcc, 0\n\ts_cbranch_scc1 2f\n\t"
                 "s_sleep 1\n\ts_sub_u32 %1, %1, 1\n\ts_cbranch_scc0 1b\n2:"
                 : "+&v"(t0), "=&s"(n) : "v"(ad), "i"(SPIN_CAP), "i"(512 * N), "is"(skip) : "vcc", "scc", "memory");
  }
  template <int C> DI void early() {
#define EARLY_TXT "s_cmp_lg_u32 %11, %12\n\ts_cbranch_scc1 1f\n\tglobal_load_dwordx4 %0, %8, %9 offset:-2048 sc1\n\tglobal_load_dwordx4 %1, %8, %9 offset:-1024 sc1\n\t" \
    "global_load_dwordx4 %2, %8, %9 sc1\n\tglobal_load_dwordx4 %3, %8, %9 offset:1024 sc1\n\tglobal_load_dwordx4 %4, %8, %9 offset:2048 sc1\n\t" \
    "global_load_dwordx4 %5, %8, %10 offset:-2048 sc1\n\tglobal_load_dwordx4 %6, %8, %10 offset:-1024 sc1\n\tglobal_load_dwordx4 %7, %8, %10 sc1\n1:"
    if constexpr (C == 0)
      asm volatile(EARLY_TXT : "=&v"(W[20]), "=&v"(W[21]), "=&v"(W[22]), "=&v"(W[23]), "=&v"(W[24]), "=&v"(W[25]), "=&v"(W[26]), "=&v"(W[27])
                   : "v"(l16), "s"(e0), "s"(e1), "s"(w4), "i"(C) : "scc", "memory");
    else
      asm volatile(EARLY_TXT : "+v"(W[20]), "+v"(W[21]), "+v"(W[22]), "+v"(W[23]), "+v"(W[24]), "+v"(W[25]), "+v"(W[26]), "+v"(W[27])
                   : "v"(l16), "s"(e0), "s"(e1), "s"(w4), "i"(C) : "scc", "memory");
  }
  DI u64 ldflag(unsigned voff, unsigned soff) const { return __builtin_bit_cast(u64, __builtin_amdgcn_raw_buffer_load_b64(rs, voff, soff, AUX_SC1)); }
  template <int T> DI void side() {
    if constexpr (T == 0) {
      relv = lds_tr2(rela, rela + 512);
      SB();
      sign();
    }
    if constexpr (T == T_PUB) publish<2, 0>(f2off);
    if constexpr (T == T_PUB + 1) stb16(rs, __builtin_bit_cast(u32x4, relv), l16, reldst);
    if constexpr (T == T_PUB + 2 || T == T_PUB + 3) stb16(rs, (u32x4){~0u, ~0u, ~0u, ~0u}, T == T_PUB + 2 ? l16 : (l16 < 256 ? l16 + 1024 : OOB), oclr);
    if constexpr (T == T_PUB2) publish<8, 1>(f2off + (WS_F3 - WS_F2));
    if constexpr (T >= T_W - 5 && T < T_W) { const unsigned ci = (T - T_W + 5) * 64 + lane_id(), row = (ci * 6554) >> 16; wv[T - T_W + 5] = row * (1280 - 160) + ci * 16; }
    if constexpr (T == T_W) {
      joined<1>(0, here(lds + L_CNT), here(0u));
    }
    if constexpr (T >= T_W && T < T_W + 19) {
      constexpr int i = T - T_W;
      __builtin_amdgcn_raw_ptr_buffer_load_lds(rw, (LAS void*)(size_t)(lds + w * L_BLK + i * 1024), 16, wv[i % 5], wdoff + (i / 5) * (256 * 1280), 0, 0);
    }
    const unsigned f2poll = WS_F2 + (rb >> 1) * 128;
    if constexpr (T == T_H - 5) pf = ldflag((lane_id() & 15) * 8, f2poll);
    if constexpr (T == T_H - 1) { wait8(pf, ws + f2poll, (lane_id() & 15) * 8, ((u64)thi << 32) | tlo); tj0 = __builtin_amdgcn_raw_buffer_load_b32(rs, min(lane_id(), 23) * 128, htouch, AUX_SC1); }
    if constexpr (T == T_H + 5) asm volatile("" ::"v"(tj0));
    if constexpr (T >= T_H && T < T_H + NH) loada<T - T_H>();
    if constexpr (T == T_F3 - 4) pf = ldflag((lane_id() & 31) * 8, WS_F3 + w * 256);
    if constexpr (T == T_F3) {
      wait8(pf, ws + WS_F3 + w * 256, (lane_id() & 31) * 8, ((u64)thi << 32) | tlo);
      sign();
      tj1 = __builtin_amdgcn_raw_buffer_load_b32(rs, min(w * 23 + min(lane_id(), 22), 179u) * 128, wtouch, AUX_SC1);
    }
    if constexpr (T == T_F3 + 6) asm volatile("" ::"v"(tj1));
    if constexpr (T == 57) zq = ldb4(rs, (lane_id() & 31) * 4, zoff);
    if constexpr (T == P_L3 || T == P_L4) {
      const u32x4 e = __builtin_amdgcn_raw_buffer_load_b128(__builtin_amdgcn_make_buffer_rsrc((void*)ltb, 0, 5120, 0x00020000), l16, here(T == P_L3 ? 3072u : 4096u), 0);
      if constexpr (T == P_L3) { scr = e[0]; wra = e[1]; so16 = e[2]; so8 = e[3]; } else { ra[0] = e[0]; ra[1] = e[1]; ra[2] = e[2]; lpo = e[3]; }
    }
    if constexpr (T == 59) { e0 = ws + w3off + 2048; e1 = e0 + 40960; oclr -= hd * 20480; w4 = w & 4; PS(e0); PS(e1); PS(oclr); PS(w4); }
    if constexpr (T == 65) { const unsigned z = here(0u); *(LAS u32x2*)(size_t)(scr + L_CN2) = (u32x2){z, z}; tj0 = here(lds + L_CNT); tj1 = z; PVV(tj0); PVV(tj1); }
    if constexpr (T == 63) { wr4 = wra + here(128 - (cb & 1) * 128); wra += here((cb & 1) * 32); PVV(wr4); PVV(wra); }
    if constexpr (T == 63 || T == 64) { readw<5 * (T - 63)>(); readw<5 * (T - 63) + 1>(); readw<5 * (T - 63) + 2>(); readw<5 * (T - 63) + 3>(); readw<5 * (T - 63) + 4>(); }
    if constexpr (T >= 62) direct<D_SLOT[T - 62], D_SLOT[T - 61]>();
  }
  DI void prep() { zq = __builtin_amdgcn_exp2f(LOG2E * __builtin_amdgcn_fmed3f(zq, -40.f, 40.f)); PVV(zq); }
  DI void o_store(const f32x16& m, float lsum) const {
    const float rl = __builtin_amdgcn_rcpf(lsum) * zq;
    stb16(rs, (u32x4){pk(m[0] * rl, m[1] * rl), pk(m[2] * rl, m[3] * rl), pk(m[4] * rl, m[5] * rl), pk(m[6] * rl, m[7] * rl)}, so16, oclr);
    stb8(rs, (u32x2){pk(m[8] * rl, m[9] * rl), pk(m[10] * rl, m[11] * rl)}, so8, oclr);
  }
  DI void last_piece() const { glds16(wd + wdoff + 3 * 256 * 1280, lpo, lds + w * L_BLK + 19 * 1024); }
  template <int J> DI void zero() {
    const f32x16 z = {0.f, 0.f, 0.f, 0.f, 0.f, 0.f, 0.f, 0.f, 0.f, 0.f, 0.f, 0.f, 0.f, 0.f, 0.f, 0.f};
    if constexpr (J == 0) nil = (s16x4){0, 0, 0, 0};
    asm volatile("" : "+v"(nil));
    o[J] = MF8(nil, nil, z);
  }
  template <int T> DI void slot() {
    constexpr int i = T >> 2, j = T & 3, c = T % 3, e = (T + 2) % 3, m = (T + 1) % 3, im = (T - 2) >> 2, jm = (T - 2) & 3;
    constexpr bool QK = T < 64, EXP = T >= 1 && T <= 64, PV = T >= 2;
    if constexpr (T == P_SW) { __builtin_amdgcn_s_setprio(0); SB(); }
    if constexpr (QK) { const f32x16 z = {0.f, 0.f, 0.f, 0.f, 0.f, 0.f, 0.f, 0.f, 0.f, 0.f, 0.f, 0.f, 0.f, 0.f, 0.f, 0.f}; s[c] = MF32(k16[i & 15], q16[j], z); }
    SB();
    side<T>();
    if constexpr (j == 0 && i + 1 < 16 && i + 1 >= NPRE) loadk<i + 1>();
    if constexpr (j == 2 && i + 1 < 16 && i + 1 >= NPRE) loadv<i + 1>();
    if constexpr (EXP) exp4<e, 0>();
    SB();
    if constexpr (QK) s[c] = MF8(k4[i & 15], q4[j], s[c]);
    SB();
    if constexpr (EXP) exp4<e, 4>();
    SB();
    if constexpr (!PV) zero<2 * T>();
    if constexpr (PV) o[jm] = smf(v0[im & 15], LO8(s[m]), o[jm], idxv);
    SB();
    if constexpr (EXP) exp4<e, 8>();
    SB();
    if constexpr (!PV) zero<2 * T + 1>();
    if constexpr (PV) o[jm] = smf(v1[im & 15], HI8(s[m]), o[jm], idxv);
    SB();
    if constexpr (EXP) exp4<e, 12>();
    if constexpr (T >= 63) {
      constexpr int jj = T - 63;
#pragma unroll
      for (int u = 0; u < 3; ++u) *(LAS f32x4*)(size_t)(scr + jj * 3072 + u * 1024) = (f32x4){o[jj][4 * u], o[jj][4 * u + 1], o[jj][4 * u + 2], o[jj][4 * u + 3]};
    }
    SB();
  }
  template <int T> DI void run() { if constexpr (T < 66) { slot<T>(); run<T + 1>(); } }
  DI bool unwritten() const {
    unsigned mx = 0;
#pragma unroll
    for (int t = 24; t < 34; ++t) { const u32x4 v = __builtin_bit_cast(u32x4, A[t]); mx = max(max(mx, v[0]), max(max(v[1], v[2]), v[3])); }
    return __any(mx == ~0u);
  }
  template <int N> DI void again() { if constexpr (N < 34) { A[N] = __builtin_bit_cast(bf16x8, ld16_raw(ws, l16 + oaddr<N>())); again<N + 1>(); } }
  DI void ready() {
    if (__builtin_expect(unwritten(), 0)) {
      unsigned n = 4096;
#pragma clang loop unroll(disable)
      do {
        __builtin_amdgcn_s_sleep(P_SL);
        again<24>();
        asm volatile("s_waitcnt vmcnt(0)" : "+v"(A[24]), "+v"(A[25]), "+v"(A[26]), "+v"(A[27]), "+v"(A[28]), "+v"(A[29]), "+v"(A[30]), "+v"(A[31]), "+v"(A[32]), "+v"(A[33])::"memory");
      } while (unwritten() && --n);
    }
  }
  template <int U, int END> DI void mm() {
    if constexpr (U < END) {
      constexpr int j = DP.j[U], ft = DP.i[U] >> 2, t = DP.i[U] & 3;
      const f32x4 z = {0.f, 0.f, 0.f, 0.f};
      acc[t][ft] = MF16(W[5 * j + ft], A[4 * j + t], j == 0 ? z : acc[t][ft]);
      SB();
      if constexpr (U == P_LR + 20) wait_vm<DP.n[U] - DP.n[0]>();
      if constexpr (U >= P_LR && U < P_LR + 40 && (U - P_LR) % 4 == 0) readw<10 + (U - P_LR) / 4>();
      direct<DP.n[U], DP.n[U + 1]>();
      if constexpr (U == 70) reach<0, 3>(scr);
      puts<DP.out[U], DP.out[U + 1]>();
      SB();
      mm<U + 1, END>();
    }
  }
  template <int F, int N> DI void reach(unsigned a) const {
    unsigned t0, n;
    asm volatile("s_mov_b32 %1, %3\n1:\n\tds_read_u8 %0, %2 offset:%4\n\ts_waitcnt lgkmcnt(0)\n\tv_cmp_ne_u32 vcc, %5, %0\n\ts_cmp_eq_u64 vcc, 0\n\ts_cbranch_scc1 2f\n\t"
                 "s_sleep 1\n\ts_sub_u32 %1, %1, 1\n\ts_cbranch_scc0 1b\n2:"
                 : "=&v"(t0), "=&s"(n) : "v"(a), "i"(SPIN_CAP), "i"(L_CN2 + F), "i"(N) : "vcc", "scc", "memory");
  }
  template <int I, int E> DI void puts() {
    if constexpr (I < E) {
      *(LAS f32x4*)(size_t)((I == 9 ? scr - (4 - w4) * 1024 : scr) + I * 1024) = acc[I & 3][I >> 2];
      if constexpr (I == 7 || I == 15 || I == 19) {
        SB();
        asm volatile("ds_add_u32 %0, %1 offset:%2" ::"v"(lds + l16), "v"(here(I == 19 ? 1u << (24 - 2 * w4) : (w4 >> 2) << (I - 7))), "i"(L_CN2 + 4) : "memory");
      }
      puts<I + 1, E>();
    }
  }
  DI void pin() const {
    asm volatile("" ::"v"(acc[0][0]), "v"(acc[0][1]), "v"(acc[0][2]), "v"(acc[0][3]), "v"(acc[0][4]), "v"(acc[1][0]), "v"(acc[1][1]), "v"(acc[1][2]), "v"(acc[1][3]), "v"(acc[1][4]),
                 "v"(acc[2][0]), "v"(acc[2][1]), "v"(acc[2][2]), "v"(acc[2][3]), "v"(acc[2][4]), "v"(acc[3][0]), "v"(acc[3][1]), "v"(acc[3][2]), "v"(acc[3][3]), "v"(acc[3][4]));
  }
};

#define P_OI 80
DI void P1::join(Attn& a, const Params& p) {
  asm volatile("" : "+v"(carry), "+s"(ta), "+s"(tc));
  ga = __builtin_bit_cast(unsigned, carry);
  t3 = lane_id() * 16; a.hd = tc[12];
  rel0 = (b < 80 ? p.wd : p.wo) + ta[0]; a.reldst = ta[1]; tb3 = p.wd + ta[2]; a.f2off = ta[3]; hso = ta[4]; a.rot = ta[5]; a.tb = ta[6]; a.oclr = ta[7];
  a.to0 = ta[8]; a.zoff = ta[9]; a.rb = ta[10]; a.cb = ta[11]; a.wdoff = ta[12]; a.w3off = ta[13]; a.wtouch = ta[14]; a.htouch = ta[15];
#pragma unroll
  for (int i = 0; i < 4; ++i) { a.qs[i] = tc[i]; a.hb[i] = tc[4 + i]; a.obs[i] = tc[8 + i]; }
}
template <int GI, int I> DI void P1::extra(Attn& a) {
  if constexpr (GI == G_TG[0] - 2 || GI == G_TG[1] - 2) {
    constexpr unsigned f = GI == G_TG[0] - 2 ? 0 : NG, n = f ? (FULLT ? 10 : 5) - NG : NG;
    if constexpr (I == 0) { to2 = min((unsigned)lane_id(), 4 * n - 1); PVV(to2); }
    if constexpr (I == 2) { t2 = __builtin_amdgcn_ubfe(t0, to2 << 3, 8) << 7; PVV(t2); }
    if constexpr (I == 3) { to2 = __umul24((to2 >> 2) + f, (FULLT ? 64 : 128) * 3072) + t2; PVV(to2); }
  }
  if constexpr (GI == G_ATT && I == 0) {
    a.l16 = t3; a.qo = lt[1][0]; a.q4o = lt[1][1]; a.ko = lt[1][2]; a.k4o = lt[1][3]; a.vo = lt[2][0]; a.vsel = lt[2][1]; a.rela = lt[2][3];
    hsa = lt[2][2] + hso; PVV(hsa);
  }
}
#define P_FI 86, 88, 90, 92, 94, 97, 98, 99
constexpr int first_at(int g) { constexpr int f[8] = {P_FI}; for (int n = 0; n < 8; ++n) if (f[n] == g) return n; return 99; }
template <int MODE, int GI> DI void P1::vmem(Attn& a) {
  if constexpr (GI == G_RD) glds16(rel0, lt[0][1], lds + L_REL + w * 1024);
  if constexpr (GI == G_RD + 1) touch(tb3, lt[0][3]);
  if constexpr (GI == G_LT[1]) lt[1] = ld16_tab<1024>(ltb, t3);
  if constexpr (GI == G_LT[2]) lt[2] = ld16_tab<2048>(ltb, t3);
  if constexpr (GI == G_ST[0]) { if constexpr (MODE == 2) stb4(rs, pq[0][0], sta[0], 0); else stb16(rs, pq[0], sta[0], 0); }
  if constexpr (GI == G_ST[1]) stb16(rs, pq[1], sta[1] + 1024, 0);
  if constexpr (GI == G_ST[2]) stb16(rs, pq[2], sta[2] + 2048, 0);
  if constexpr (GI == G_ST[3]) stb16(rs, pq[3], sta[3] + 3072, 0);
  if constexpr (GI == bar_at(B_F1)) stb8(rs, (u32x2){(unsigned)tag, (unsigned)(tag >> 32)}, (t3 << 21) + f1off, 0);
  if constexpr (GI == G_POLL) pf = __builtin_bit_cast(u64, ld8_raw(ws + WS_F1 + w * 256, lt[0][2]));
  if constexpr (GI >= G_ATT) a.first<first_at(GI)>();
  if constexpr (GI == 96) h_store<0>();
  SB();
  if constexpr (GI < 40) loads<PL.cum[GI], PL.cum[GI + 1]>();
}

DI const char* ptr(unsigned lo, unsigned hi) { return (const char*)(((u64)hi << 32) | lo); }
DI void P1::head(Stamps& st, Attn& a, Params& p) {
  u32x16 ka; u32x2 kb; const char* pc; unsigned span;
  asm volatile("s_load_dwordx16 %0, %4, 0x0\n\ts_load_dwordx2 %1, %4, 0x40\n\ts_getpc_b64 %2\n.Lcode0:\n\ts_mov_b32 %3, .Lcode1-.Lcode0"
               : "=&s"(ka), "=&s"(kb), "=&s"(pc), "=&s"(span) : "s"(__builtin_amdgcn_kernarg_segment_ptr()));
  __builtin_amdgcn_s_setprio(3);
  const unsigned t = threadIdx.x, l = t & 63, x1 = b & 1, x2 = (b >> 1) & 3, idx = b >> 3, i7 = idx & 7, i2 = b >> 6;
  touch(pc, min((min(l, 4u) << 7) + here((i7 * 8 + w) * 640), span));
  SB();
  const unsigned R = 8 * x1 + i7, J = 4 * x2 + i2, g = (t >> 4) & 3;
  const bool gate = J == 15, lo = w < 4;
  w2k = w * 2048;
  xo0 = __umul24((t >> 3) & 7, 1280) + (((t ^ g) << 4) & 112);
  xo1 = (xo0 ^ 64) + (10240 - 1024);
  const unsigned ng = (int)here(J - 15) >> 31;
  wo1 = __umul24(g, ng & 3840) + (((t ^ ((t >> 3) & (ng & 14))) << 4) & (112 | (ng & 128)));
  wo0 = wo1 + 1024;
  wo1 += ng & 15360;
  unsigned ox = (R * 8 + w) * 20480, ow = ng & (J * 256 + w * 30720);
  PVV(xo0); PVV(xo1); PVV(wo0); PVV(wo1); PS(ox); PS(ow); PS(w2k);
  SB();
  const unsigned cap = here(7 ^ ((w >> 1) & b & x2 & 2)), shx = (x1 * 128 + x2 * 32 + idx) * 4 + w, shw = x1 * 32 + idx;
  auto lines = [&](unsigned A, unsigned B, unsigned& vo, unsigned& so) {
    const unsigned n = B - A, j = min(l, 2 * n - 1), k = j + here((w & 3) * 2 * n);
    const unsigned vx = __umul24(j & 1, 1280 - 64) + (j << 6), vw = __umul24(k >> 3, 3840) + (min(k & 7, cap) << 7);
    const unsigned oa = here(shx * 2560 + A * 128), ob = here((shw * n + A * 64) * 3840 + x2 * 1024);
    vo = lo ? vx : vw; so = lo ? oa : ob;
    PVV(vo); PS(so);
  };
  unsigned to0, o1;
  lines(TS0, TS1, to0, o1);
  SB();
  asm volatile("s_waitcnt lgkmcnt(0)" : "+s"(ka), "+s"(kb));
  p.x = ptr(ka[0], ka[1]); p.wqkv = ptr(ka[2], ka[3]); p.wo = ptr(ka[4], ka[5]); p.wg = ptr(ka[6], ka[7]); p.wu = ptr(ka[8], ka[9]);
  p.wd = ptr(ka[10], ka[11]); p.gw = ptr(ka[12], ka[13]); p.out = (char*)ptr(ka[14], ka[15]); p.ws = (char*)ptr(kb[0], kb[1]);
  STAMP(1);
  const char* tsrc = lo ? p.x : p.wqkv;
  if constexpr (TPOS == -1) touch(tsrc + o1, to0);
  xb = p.x + ox;
  pair<0>();
  SB();
  wqb = (gate ? p.gw : p.wqkv) + ((long long)ow - 1024);
  pair<2>();
  SB();
  if constexpr (TPOS == 0) touch(tsrc + o1, to0);
  __builtin_amdgcn_s_setprio(0);
  STAMP(2);
  ws = p.ws;
  asm volatile("s_load_dwordx2 %0, %3, 0x0\n\ts_load_dwordx16 %1, %4, 0x0\n\ts_load_dwordx16 %2, %4, 0x40"
               : "=&s"(epoch), "=&s"(ta), "=&s"(tc) : "s"(ws + WS_E), "s"((const char*)TAB + (b * 8 + w) * 128) : "memory");
  wst = gate ? 128 : 64 * 3840;
  if constexpr (EARLY1 == 1) { pair<4>(); pair<6>(); }
  SB();
  {
    const unsigned l16 = l << 4, rq = ((t >> 1) & 24) | ((t >> 2) & 3), cg = ((t >> 4) ^ t) & 16;
    xa[0][0] = (((t & 15) << 7) | (((g ^ (t >> 1)) & 7) << 4)) + (16384 + (w & 3) * 4096);
    xa[0][1] = xa[0][0] ^ 64;
    wa[0][0] = (rq << 8) | (((t & 15) | cg) << 3);
    ga = __umul24(rq, 192) + (((cg << 1) | ((t & 3) << 3)) + 98304);
#pragma unroll
    for (int n = 1; n < 4; ++n) wa[0][n] = wa[0][0] ^ (n << 5);
    xa[1][0] = xa[0][0] + 65536; xa[1][1] = xa[0][1] + 65536;
#pragma unroll
    for (int n = 0; n < 4; ++n) wa[1][n] = wa[0][n] + 65536;
    go0 = __umul24(__umul24(l, 171) >> 11, 2880) + l16;
    go1 = (__umul24((__umul24(l, 171) + 64 * 171) >> 11, 2880) + l16) ^ (l & 32);
    go2 = (__umul24((__umul24(l, 171) + 128 * 171) >> 11, 2880) + l16) ^ 32;
    asm volatile("" : "+v"(xa[0][0]), "+v"(xa[0][1]), "+v"(wa[0][0]), "+v"(wa[0][1]), "+v"(wa[0][2]), "+v"(wa[0][3]), "+v"(go0), "+v"(go1), "+v"(go2), "+v"(ga));
    asm volatile("" : "+v"(xa[1][0]), "+v"(xa[1][1]), "+v"(wa[1][0]), "+v"(wa[1][1]), "+v"(wa[1][2]), "+v"(wa[1][3]));
  }
  SB();
  if constexpr (TPOS == 1) touch(tsrc + o1, to0);
  if constexpr (EARLY1 == 2) { pair<4>(); pair<6>(); }
  SB();
  {
    const unsigned v = w, v3 = v & 3;
    const char* gu = lo ? p.wg : p.wu;
    tb2 = gu + (((FULLT ? idx : shw) * 2 + (v3 >> 1)) * 3072);
    { const unsigned k = 4 * x2 + 2 * (v & 1); t0 = (3 * gcol(k) >> 1) | ((3 * gcol(k + 1) >> 1) << 16); t0 += (t0 + 0x10001) << 8; PS(t0); }
    unsigned o2;
    lines(TS1, 10, to1, o2);
    tb1 = tsrc + o2;
    PS(tb1);
    ltb = (const char*)&LTAB + v * 5120;
    PS(ltb);
    touch(ltb, (min(l, 4u) + here(i7 * 5)) << 7);
    *(LAS u32x4*)(size_t)(lds + L_CNT) = (u32x4){0u, 0u, 0u, 0u};
    gub = gu + (gcol(J) * 192 + v3 * (16 * 3072));
    gdst = v * 3072;
    f1off = v == 0 ? WS_F1 + b * 8 : OOB;
    rt = R * 4 + v3;
    PS(tb2); PS(gub); PS(gdst); PS(f1off); PS(rt);
  }
  SB();
  STAMP(3);
  if constexpr (EARLY1 == 0) { pair<4>(); pair<6>(); }
  if constexpr (TPOS == 2) touch(tsrc + o1, to0);
  ahead<0>();
  SB();
}

__global__ __launch_bounds__(512, 2) void fwd(Params) {
  Stamps st; STAMP(0);
  const unsigned w = __builtin_amdgcn_readfirstlane(threadIdx.x >> 6), b = blockIdx.x;
  Params p;
  Attn a;
  P1 s;
  s.w = w; s.b = b;
  s.head(st, a, p);
  char* ws = p.ws;
  const rsrc_t rs = __builtin_amdgcn_make_buffer_rsrc((void*)ws, 0, (int)WS_END, 0x00020000);
  unsigned tlo, thi;
  {
    const unsigned J = 2 * (b & 6) + (b >> 6);
    s.rs = rs;
    if (J < 10) s.qkv<0>(st, a, J);
    else if (J < 15) s.qkv<1>(st, a, J);
    else s.qkv<2>(st, a, J);
    s.join(a, p);
    a.rs = rs; a.ws = ws; a.w = w; a.ltb = s.ltb;
    a.idxv = 0xDDDD;
    a.wd = p.wd; a.rw = __builtin_amdgcn_make_buffer_rsrc((void*)p.wd, 0, 1536 * 1280, 0x00020000);
    STAMP(7);
    tlo = (unsigned)s.tag; thi = (unsigned)(s.tag >> 32);
    a.tlo = tlo; a.thi = thi;
    s.run<0, 56, G_ATT>(a);
    STAMP(8);
    s.run<0, G_ATT, G_ATT + 1>(a);
    STAMP(9);
    s.run<0, G_ATT + 1, 100>(a);
    STAMP(10);
    s.h_store<1>();
    s.silu_a<2, 0>(); s.silu_b<0>(); s.silu_a<2, 1>(); s.silu_b<1>(); s.silu_a<2, 2>(); s.silu_b<2>(); s.silu_a<2, 3>(); s.silu_b<3>();
    s.h_store<2>();
  }
  STAMP(11);
  if (w >= 4) __builtin_amdgcn_s_setprio(1);
  a.run<0>();
  STAMP(12);
  a.direct<D_SLOT[4], D_SLOT[5]>();
  PVV(a.ra[0]);
  a.joined<2>(a.w4, a.tj0, a.tj1);
  a.early<0>();
  __builtin_amdgcn_s_setprio(2);
  SB();
  bar_lds();
  STAMP(13);
  {
    f32x4 t[9];
#pragma unroll
    for (int i = 0; i < 9; ++i) t[i] = *(const LAS f32x4*)(size_t)(a.ra[i % 3] + (2 - i / 3) * 1024);
    SB();
    a.prep();
    SB();
    f32x16 m = a.o[3];
#pragma unroll
    for (int u = 2; u >= 0; --u) {
      const f32x4 c = ((f32x4){m[4 * u], m[4 * u + 1], m[4 * u + 2], m[4 * u + 3]} + t[6 - 3 * u]) + (t[7 - 3 * u] + t[8 - 3 * u]);
      m[4 * u] = c[0]; m[4 * u + 1] = c[1]; m[4 * u + 2] = c[2]; m[4 * u + 3] = c[3];
    }
    SB();
    asm volatile("ds_add_u32 %1, %0 offset:3072\n\tds_add_u32 %2, %0 offset:6144\n\tds_add_u32 %3, %0 offset:9216" ::"v"(here(1u)), "v"(a.ra[0]), "v"(a.ra[1]), "v"(a.ra[2]) : "memory");
    const float lsum = upper(m[8]);
    const f32x2 all = (((f32x2){m[0], m[1]} + (f32x2){m[2], m[3]}) + ((f32x2){m[4], m[5]} + (f32x2){m[6], m[7]})) + ((f32x2){m[8], m[9]} + (f32x2){m[10], m[11]});
    const float chk = __builtin_fmaf(all[0] + all[1], 0.f, lsum);
    if (__builtin_expect(__builtin_amdgcn_ballot_w64(!(__builtin_amdgcn_fmed3f(chk, 0x1p-20f, 0x1p120f) == chk)) != 0, 0)) {
      f32x16 o;
#pragma unroll
      for (int i = 0; i < 16; ++i) o[i] = 0.f;
      float mx = -__builtin_inff();
      const bf16x8 q16 = ldb16(rs, a.l16 + a.hd * 1536, a.qs[3]);
      const s16x4 q4 = ldb8(rs, a.l16 < 512 ? a.l16 + 1024 + a.hd * 8 : OOB, a.qs[3]);
#pragma unroll 1
      for (int t = 0; t < 64; ++t) {
        const unsigned so = a.tb - (here(w) >> 1) * (16 * 5120) + t * 5120;
        const bf16x8 k16 = ldb16(rs, a.ko, so);
        const s16x4 k4 = ldb8(rs, a.k4o, so);
        const unsigned va = a.vo + __umul24(a.vsel, so);
        const bf16x8 v0 = ldb16(rs, va, 0), v1 = ldb16(rs, va + 128, 0);
        f32x16 sv;
#pragma unroll
        for (int i = 0; i < 16; ++i) sv[i] = 0.f;
        sv = MF32(k16, q16, sv);
        sv = MF8(k4, q4, sv);
        float tm = sv[0];
#pragma unroll
        for (int i = 1; i < 16; ++i) tm = fmaxf(tm, sv[i]);
        tm = fmaxf(tm, __shfl_xor(tm, 32));
        const float mn = fmaxf(mx, tm);
        const float alpha = __builtin_amdgcn_exp2f(mx - mn);
        mx = mn;
#pragma unroll
        for (int i = 0; i < 16; ++i) { o[i] *= alpha; sv[i] = __builtin_amdgcn_exp2f(sv[i] - mn); }
        o = smf(v0, LO8(sv), o, a.idxv);
        o = smf(v1, HI8(sv), o, a.idxv);
      }
      a.o_store(o, upper(o[8]));
    } else a.o_store(m, lsum);
  }
  STAMP(14);
  __builtin_amdgcn_s_setprio(0);
  a.early<4>();
  a.last_piece();
  SB();
  a.mm<0, 20 * TS>();
  a.pin();
  STAMP(15);
  a.ready();
  SB();
  a.mm<20 * TS, NM>();
  a.puts<DP.out[NM], 20>();
  STAMP(16);
  if (w < 4) {
    const unsigned v = here(w), l16 = a.l16;
    float zo = ldb4(rs, (l16 & 255) >> 2, WS_G + a.rb * 256 + v * 64);
    const rsrc_t ro = __builtin_amdgcn_make_buffer_rsrc((void*)p.out, 0, 2048 * 1280, 0x00020000);
    const unsigned orow = __umul24(l16 & 255, 80) + here((a.rb * 64 + v * 16) * 1280 + a.cb * 160);
    unsigned o16 = orow + ((l16 & 256) >> 3) + ((l16 >> 9) << 4) + here((a.cb & 1) * 32), o8 = orow + here(128 - (a.cb & 1) * 128) + ((l16 >> 8) << 3);
    unsigned pa[4], pb[4];
#pragma unroll
    for (int d = 0; d < 4; ++d) { pa[d] = lds + l16 + ((v + d) & 3) * L_BLK; pb[d] = pa[d] + 4 * L_BLK; PVV(pa[d]); PVV(pb[d]); }
    PVV(o16); PVV(o8);
    auto rd = [](unsigned ad) { return *(const LAS f32x4*)(size_t)ad; };
    a.reach<7, 4>(lds + l16);
    f32x4 c[5];
#pragma unroll
    for (int ft = 0; ft < 5; ++ft) c[ft] = (a.acc[1][ft] + rd(pa[1] + ft * 4096)) + (rd(pa[2] + ft * 4096 + 3072) + rd(pa[3] + ft * 4096 + 2048));
    zo = __builtin_amdgcn_rcpf(1.0f + __builtin_amdgcn_exp2f(LOG2E * __builtin_amdgcn_fmed3f(zo, -40.f, 40.f)));
    asm volatile("" : "+v"(c[0]), "+v"(c[1]), "+v"(c[2]), "+v"(c[3]), "+v"(c[4]), "+v"(zo));
    auto fin = [&](int ft) {
      const f32x4 e = (c[ft] + ((rd(pb[0] + ft * 4096 + 3072) + rd(pb[1] + ft * 4096 + 2048)) + (rd(pb[2] + ft * 4096 + 1024) + rd(pb[3] + ft * 4096)))) * zo;
      return (u32x2){pk(e[0], e[1]), pk(e[2], e[3])};
    };
    a.reach<4, 4>(lds + l16);
    { const u32x2 c0 = fin(0), c1 = fin(1); __builtin_amdgcn_raw_buffer_store_b128(pair_rows(c0[0], c0[1], c1[0], c1[1]), ro, o16, 0, OUT_AUX); }
    a.reach<5, 4>(lds + l16);
    { const u32x2 c0 = fin(2), c1 = fin(3); __builtin_amdgcn_raw_buffer_store_b128(pair_rows(c0[0], c0[1], c1[0], c1[1]), ro, o16 + 64, 0, OUT_AUX); }
    asm volatile("s_barrier" ::: "memory");
    __builtin_amdgcn_raw_buffer_store_b64(fin(4), ro, o8, 0, OUT_AUX);
  } else asm volatile("s_waitcnt lgkmcnt(0)\n\ts_barrier" ::: "memory");
  if (b == 0 && w == 0 && a.l16 == 0) set_flag(ws, WS_E, tlo, thi);
  asm volatile(".Lcode1:");
  STAMP(17);
  STAMP(18);
  STAMP_OUT(0, 19);
}

extern "C" void kernel_launch(void* const* d_in, const int* in_sizes, int n_in, void* d_out, int out_size, void* d_ws, size_t ws_size,
                              hipStream_t stream) {
  Params p;
  p.x = (const char*)d_in[0]; p.wqkv = (const char*)d_in[1]; p.wo = (const char*)d_in[2];
  p.wg = (const char*)d_in[4]; p.wu = (const char*)d_in[5]; p.wd = (const char*)d_in[6]; p.gw = (const char*)d_in[7];
  p.out = (char*)d_out; p.ws = (char*)d_ws;
  (void)hipFuncSetAttribute((const void*)fwd, hipFuncAttributeMaxDynamicSharedMemorySize, LDS_BYTES);
  fwd<<<256, 512, LDS_BYTES, stream>>>(p);
}
```
